```python
import torch

def _softmax(x, axis=-1):
    return torch.softmax(x, dim=axis)
def _gelu(x):
    return torch.nn.functional.gelu(x, approximate="tanh")
def _silu(x):
    return torch.nn.functional.silu(x)
def _sigmoid(x):
    return torch.sigmoid(x)
def _layernorm(x, axis=-1, eps=1e-5):
    m = x.mean(dim=axis, keepdim=True)
    v = x.var(dim=axis, keepdim=True, unbiased=False)
    return (x - m) / torch.sqrt(v + eps)
def _scatter_add(tgt, idx, src, axis=0):
    return tgt.clone().index_add_(axis, idx, src)
def _segment_sum(src, seg, n):
    out = torch.zeros((n,) + src.shape[1:], dtype=src.dtype)
    return out.index_add_(0, seg, src)
def _pad(x, pw):
    flat = tuple(v for pair in reversed(pw) for v in pair)
    return torch.nn.functional.pad(x, flat)

kernel_name = 'sequential_preLN__spectral_gfnet__mixer_token_mlp__prompt'

def setup_inputs():

    _w = (lambda a: (lambda t: t.to(torch.bfloat16) if t.is_floating_point() else t)(torch.as_tensor(a)))
    def _A_setup():
        B, S, D = 1, 2048, 448
        Df = D // 2 + 1
        return {
            "x": _w((lambda *s: torch.randn(*s, dtype=torch.bfloat16) * 0.05)(B, S, D)),
            "w_r": _w((lambda *s: torch.randn(*s, dtype=torch.bfloat16) * 0.4)(S, Df) * 0.02),
            "w_i": _w((lambda *s: torch.randn(*s, dtype=torch.bfloat16) * 0.4)(S, Df) * 0.02),
        }
    _A_in = _A_setup()

    def _B_setup():
        B, S, D, F = 1, 2048, 448, 1792
        return {
            "x": _w((lambda *s: torch.randn(*s, dtype=torch.bfloat16) * 0.05)(B, S, D)),
            "w1": _w((lambda *s: torch.randn(*s, dtype=torch.bfloat16) * 0.4)(S, F) * (S ** -0.5)),
            "b1": _w((lambda *s: torch.randn(*s, dtype=torch.bfloat16) * 3.2)(F) * 0.02),
            "w2": _w((lambda *s: torch.randn(*s, dtype=torch.bfloat16) * 0.2)(F, S) * (F ** -0.5)),
            "b2": _w((lambda *s: torch.randn(*s, dtype=torch.bfloat16) * 0.4)(S) * 0.02),
        }
    _B_in = _B_setup()

    return {
        "A_x": _A_in["x"],
        "A_w_r": _A_in["w_r"],
        "A_w_i": _A_in["w_i"],
        "B_x": _B_in["x"],
        "B_w1": _B_in["w1"],
        "B_b1": _B_in["b1"],
        "B_w2": _B_in["w2"],
        "B_b2": _B_in["b2"],
    }

def reference(A_x, A_w_r, A_w_i, B_x, B_w1, B_b1, B_w2, B_b2):
    def _A_ref(x, w_r, w_i):
        B, S, D = x.shape
        X = (lambda x, axes=(-2,-1): torch.fft.rfft2(x.float(), dim=axes))(x, axes=(-2, -1))
        W = w_r + 1j * w_i
        X = X * W
        y = (lambda x, s, axes=(-2,-1): torch.fft.irfft2(x, s=s, dim=axes).to(torch.bfloat16))(X, (S, D), axes=(-2, -1))
        return y

    def _B_ref(x, w1, b1, w2, b2):
        B, S, D = x.shape
        h = _layernorm(x, axis=-1)
        u = torch.einsum("bsd,sf->bdf", h, w1) + b1
        u = _gelu(u)
        y = torch.einsum("bdf,fs->bds", u, w2) + b2
        return (lambda x, *d: x.permute(*d))(y, 0, 2, 1)

    x = A_x
    h = x + _A_ref(_layernorm(x, axis=-1), A_w_r, A_w_i)
    h2 = h + _B_ref(_layernorm(h, axis=-1), B_w1, B_b1, B_w2, B_b2)
    return h2
```

```cpp
#include <hip/hip_runtime.h>
#include <hip/hip_bf16.h>
#include <stdint.h>

typedef unsigned short u16;
#define S_ 2048
#define D_ 448
#define DF 225
#define F_ 1792

__device__ __forceinline__ float bf2f(u16 v) { return __uint_as_float(((unsigned)v) << 16); }
__device__ __forceinline__ u16 f2bf(float f) {
    unsigned u = __float_as_uint(f);
    return (u16)((u + 0x7FFFu + ((u >> 16) & 1u)) >> 16);
}
__device__ __forceinline__ float rbf(float f) { return bf2f(f2bf(f)); }

__device__ float block_sum(float v, float* red) {
    for (int o = 32; o > 0; o >>= 1) v += __shfl_xor(v, o);
    __syncthreads();
    if ((threadIdx.x & 63) == 0) red[threadIdx.x >> 6] = v;
    __syncthreads();
    float t = 0;
    for (int i = 0; i < (int)(blockDim.x >> 6); ++i) t += red[i];
    return t;
}

__device__ float ln_elem(float x, float* red) {
    float mean = block_sum(x, red) * (1.0f / D_);
    float dv = x - mean;
    float var = block_sum(dv * dv, red) * (1.0f / D_);
    float m = rbf(mean), v = rbf(var);
    float den = rbf(sqrtf(rbf(v + 1e-5f)));
    return rbf(rbf(x - m) / den);
}

__global__ void k1(const u16* x, float2* X1) {
    __shared__ float row[D_];
    __shared__ float red[8];
    __shared__ float2 tw[D_];
    int s = blockIdx.x, t = threadIdx.x;
    float xv = bf2f(x[s * D_ + t]);
    row[t] = ln_elem(xv, red);
    float sn, cs;
    sincospif(-2.0f * t / D_, &sn, &cs);
    tw[t] = make_float2(cs, sn);
    __syncthreads();
    if (t < DF) {
        float re = 0, im = 0;
        int idx = 0;
        for (int d = 0; d < D_; ++d) {
            re += row[d] * tw[idx].x; im += row[d] * tw[idx].y;
            idx += t; if (idx >= D_) idx -= D_;
        }
        X1[s * DF + t] = make_float2(re, im);
    }
}
__global__ void k2(const float2* in, float2* out, const u16* wr, const u16* wi, int fwd) {
    __shared__ float2 tw[S_];
    int ks = blockIdx.x, t = threadIdx.x;
    for (int i = t; i < S_; i += blockDim.x) {
        float sn, cs;
        sincospif((fwd ? -2.0f : 2.0f) * i / S_, &sn, &cs);
        tw[i] = make_float2(cs, sn);
    }
    __syncthreads();
    if (t < DF) {
        float re = 0, im = 0;
        for (int s = 0; s < S_; ++s) {
            float2 w = tw[(s * ks) & (S_ - 1)];
            float2 v = in[s * DF + t];
            re += v.x * w.x - v.y * w.y;
            im += v.x * w.y + v.y * w.x;
        }
        if (fwd) {
            float a = bf2f(wr[ks * DF + t]), b = bf2f(wi[ks * DF + t]);
            float r2 = re * a - im * b, i2 = re * b + im * a;
            re = r2; im = i2;
        }
        out[ks * DF + t] = make_float2(re, im);
    }
}
__global__ void k3(const float2* Z, const u16* x, u16* h, u16* hn) {
    __shared__ float2 row[DF];
    __shared__ float red[8];
    __shared__ float2 tw[D_];
    int s = blockIdx.x, t = threadIdx.x;
    if (t < DF) row[t] = Z[s * DF + t];
    float sn, cs;
    sincospif(2.0f * t / D_, &sn, &cs);
    tw[t] = make_float2(cs, sn);
    __syncthreads();
    float acc = 0;
    int idx = t;
    for (int k = 1; k < DF - 1; ++k) {
        acc += row[k].x * tw[idx].x - row[k].y * tw[idx].y;
        idx += t; if (idx >= D_) idx -= D_;
    }
    acc = 2.0f * acc + row[0].x + ((t & 1) ? -row[DF - 1].x : row[DF - 1].x);
    acc *= 1.0f / ((float)S_ * D_);
    float y = rbf(acc);
    float hv = rbf(bf2f(x[s * D_ + t]) + y);
    h[s * D_ + t] = f2bf(hv);
    float a = ln_elem(hv, red);
    float b = ln_elem(a, red);
    hn[s * D_ + t] = f2bf(b);
}
__global__ void k4(const u16* hn, const u16* w1, const u16* b1, u16* u) {
    int f = blockIdx.x * 256 + threadIdx.x, d = blockIdx.y;
    float acc = 0;
    for (int s = 0; s < S_; ++s) acc += bf2f(hn[s * D_ + d]) * bf2f(w1[s * F_ + f]);
    float v = rbf(rbf(acc) + bf2f(b1[f]));
    float g = 0.5f * v * (1.0f + tanhf(0.7978845608028654f * (v + 0.044715f * v * v * v)));
    u[d * F_ + f] = f2bf(g);
}
__global__ void k5(const u16* u, const u16* w2, const u16* b2, const u16* h, u16* out) {
    int s = blockIdx.x * 256 + threadIdx.x, d = blockIdx.y;
    float acc = 0;
    for (int f = 0; f < F_; ++f) acc += bf2f(u[d * F_ + f]) * bf2f(w2[f * S_ + s]);
    float v = rbf(rbf(acc) + bf2f(b2[s]));
    out[s * D_ + d] = f2bf(bf2f(h[s * D_ + d]) + v);
}

extern "C" void kernel_launch(void* const* d_in, const int* in_sizes, int n_in,
                              void* d_out, int out_size, void* d_ws, size_t ws_size,
                              hipStream_t stream) {
    const u16* x = (const u16*)d_in[0];
    const u16* wr = (const u16*)d_in[1];
    const u16* wi = (const u16*)d_in[2];
    const u16* w1 = (const u16*)d_in[4];
    const u16* b1 = (const u16*)d_in[5];
    const u16* w2 = (const u16*)d_in[6];
    const u16* b2 = (const u16*)d_in[7];
    char* ws = (char*)d_ws;
    float2* X1 = (float2*)ws;
    float2* X2 = (float2*)(ws + (4 << 20));
    u16* h = (u16*)(ws + (8 << 20));
    u16* hn = (u16*)(ws + (10 << 20));
    u16* u = (u16*)(ws + (12 << 20));
    k1<<<S_, D_, 0, stream>>>(x, X1);
    k2<<<S_, 256, 0, stream>>>(X1, X2, wr, wi, 1);
    k2<<<S_, 256, 0, stream>>>(X2, X1, wr, wi, 0);
    k3<<<S_, D_, 0, stream>>>(X1, x, h, hn);
    k4<<<dim3(F_ / 256, D_), 256, 0, stream>>>(hn, w1, b1, u);
    k5<<<dim3(S_ / 256, D_), 256, 0, stream>>>(u, w2, b2, h, (u16*)d_out);
}
```

```cpp
#include <hip/hip_runtime.h>
#include <hip/hip_bf16.h>
#include <stdint.h>

typedef unsigned short u16;
typedef float cf __attribute__((ext_vector_type(2)));
typedef float f4 __attribute__((ext_vector_type(4)));
typedef unsigned u4 __attribute__((ext_vector_type(4)));
typedef unsigned u2 __attribute__((ext_vector_type(2)));
typedef __bf16 bf2 __attribute__((ext_vector_type(2)));
typedef __bf16 bf8 __attribute__((ext_vector_type(8)));

#define SN 2048
#define DN 448
#define DFQ 225
#define FN 1792
#define TROW 240

constexpr double cx_pi = 3.14159265358979323846264338327950288;
constexpr double cx_sin_t(double x) { double x2 = x * x, t = x, s = x; for (int i = 1; i < 12; ++i) { t *= -x2 / ((2 * i) * (2 * i + 1)); s += t; } return s; }
constexpr double cx_cos_t(double x) { double x2 = x * x, t = 1, s = 1; for (int i = 1; i < 12; ++i) { t *= -x2 / ((2 * i - 1) * (2 * i)); s += t; } return s; }
constexpr double cx_c2048(int n) {
    n &= 2047; int q = n >> 9, r = n & 511; double a = 2.0 * cx_pi * r / 2048.0;
    double c = 0, s = 0;
    if (r <= 256) { c = cx_cos_t(a); s = cx_sin_t(a); } else { double b = 2.0 * cx_pi * (512 - r) / 2048.0; c = cx_sin_t(b); s = cx_cos_t(b); }
    return q == 0 ? c : q == 1 ? -s : q == 2 ? -c : s;
}
constexpr double cx_s2048(int n) { return cx_c2048(n - 512); }
struct Tables {
    float ws[256][8][2];
    float w64[8][8][2];
    float w256[64][4][2];
};
struct CosTab { float c[2048]; };
constexpr CosTab make_cos() { CosTab t{}; for (int i = 0; i < 2048; ++i) t.c[i] = (float)cx_c2048(i); return t; }
constexpr CosTab kCos = make_cos();
constexpr Tables make_tables() {
    Tables t{};
    for (int s = 0; s < 256; ++s) for (int q = 0; q < 8; ++q) { int n = (s * q) & 2047; t.ws[s][q][0] = kCos.c[n]; t.ws[s][q][1] = -kCos.c[(n - 512) & 2047]; }
    for (int i = 0; i < 8; ++i) for (int e = 0; e < 8; ++e) { int n = (i * e * 32) & 2047; t.w64[i][e][0] = kCos.c[n]; t.w64[i][e][1] = -kCos.c[(n - 512) & 2047]; }
    for (int r = 0; r < 64; ++r) for (int b = 0; b < 4; ++b) { int n = (r * b * 8) & 2047; t.w256[r][b][0] = kCos.c[n]; t.w256[r][b][1] = -kCos.c[(n - 512) & 2047]; }
    return t;
}
__device__ const __attribute__((aligned(64))) Tables kTab = make_tables();

#define WSYNC() do { __builtin_amdgcn_fence(__ATOMIC_ACQ_REL, "wavefront"); __builtin_amdgcn_wave_barrier(); } while (0)
__device__ __forceinline__ cf cfma(cf a, cf b, cf c) { return __builtin_elementwise_fma(a, b, c); }
__device__ __forceinline__ cf addi(cf a, cf b) { return cfma(b.yx, cf{-1.f, 1.f}, a); }
__device__ __forceinline__ cf subi(cf a, cf b) { return cfma(b.yx, cf{1.f, -1.f}, a); }
struct Tw { cf w; cf n; };
__device__ __forceinline__ Tw mktw(cf w) { Tw t; t.w = w; t.n = cf{-w.y, w.y}; return t; }
__device__ __forceinline__ cf cmul(cf a, const Tw& t) { return cfma(a.yx, t.n, a * t.w.xx); }
__device__ __forceinline__ cf cmulc(cf a, const Tw& t) { return cfma(a.yx, t.n.yx, a * t.w.xx); }

template <bool INV> __device__ __forceinline__ cf rot_m(cf a, cf b) { return INV ? addi(a, b) : subi(a, b); }
template <bool INV> __device__ __forceinline__ cf rot_p(cf a, cf b) { return INV ? subi(a, b) : addi(a, b); }

template <bool INV> __device__ __forceinline__ void dft8(cf (&x)[8]) {
    const float R = 0.70710678118654752440f;
    cf a0 = x[0] + x[4], a4 = x[0] - x[4], a1 = x[1] + x[5], a5 = x[1] - x[5];
    cf a2 = x[2] + x[6], a6 = x[2] - x[6], a3 = x[3] + x[7], a7 = x[3] - x[7];
    cf b0 = a0 + a2, b2 = a0 - a2, b1 = a1 + a3, b3 = a1 - a3;
    cf c0 = rot_m<INV>(a4, a6), c2 = rot_p<INV>(a4, a6);
    cf e = rot_m<INV>(a5, a7), f = rot_p<INV>(a5, a7);
    cf g = rot_m<INV>(e, e), h = rot_m<INV>(f, f);
    x[0] = b0 + b1; x[4] = b0 - b1;
    x[2] = rot_m<INV>(b2, b3); x[6] = rot_p<INV>(b2, b3);
    x[1] = cfma(g, cf{R, R}, c0); x[5] = cfma(g, cf{-R, -R}, c0);
    x[3] = cfma(h.yx, INV ? cf{-R, R} : cf{R, -R}, c2);
    x[7] = cfma(h.yx, INV ? cf{R, -R} : cf{-R, R}, c2);
}
template <bool INV> __device__ __forceinline__ void dft4(cf& x0, cf& x1, cf& x2, cf& x3) {
    cf a0 = x0 + x2, a2 = x0 - x2, a1 = x1 + x3, a3 = x1 - x3;
    x0 = a0 + a1; x2 = a0 - a1; x1 = rot_m<INV>(a2, a3); x3 = rot_p<INV>(a2, a3);
}
template <bool INV> __device__ __forceinline__ void dft7(cf (&x)[7]) {
    const float C1 = 0.62348980185873353053f, C2 = -0.22252093395631440429f, C3 = -0.90096886790241912624f;
    const float S1 = 0.78183148246802980871f, S2 = 0.97492791218182360702f, S3 = 0.43388373911755812048f;
    cf s1 = x[1] + x[6], d1 = x[1] - x[6], s2 = x[2] + x[5], d2 = x[2] - x[5], s3 = x[3] + x[4], d3 = x[3] - x[4];
    cf x0 = x[0];
    cf A1 = cfma(s3, cf{C3, C3}, cfma(s2, cf{C2, C2}, cfma(s1, cf{C1, C1}, x0)));
    cf A2 = cfma(s3, cf{C1, C1}, cfma(s2, cf{C3, C3}, cfma(s1, cf{C2, C2}, x0)));
    cf A3 = cfma(s3, cf{C2, C2}, cfma(s2, cf{C1, C1}, cfma(s1, cf{C3, C3}, x0)));
    cf B1 = cfma(d3, cf{S3, S3}, cfma(d2, cf{S2, S2}, d1 * cf{S1, S1}));
    cf B2 = cfma(d3, cf{-S1, -S1}, cfma(d2, cf{-S3, -S3}, d1 * cf{S2, S2}));
    cf B3 = cfma(d3, cf{S2, S2}, cfma(d2, cf{-S1, -S1}, d1 * cf{S3, S3}));
    x[0] = (x0 + s1) + (s2 + s3);
    x[1] = rot_m<INV>(A1, B1); x[6] = rot_p<INV>(A1, B1);
    x[2] = rot_m<INV>(A2, B2); x[5] = rot_p<INV>(A2, B2);
    x[3] = rot_m<INV>(A3, B3); x[4] = rot_p<INV>(A3, B3);
}

__device__ __forceinline__ unsigned pk_bf16(cf v) { return __builtin_bit_cast(unsigned, __builtin_convertvector(v, bf2)); }
__device__ __forceinline__ cf unpk_bf16(unsigned p) { return cf{__uint_as_float(p << 16), __uint_as_float(p & 0xffff0000u)}; }
__device__ __forceinline__ cf rbf2(cf v) { return unpk_bf16(pk_bf16(v)); }

template <int CTRL, int RM> __device__ __forceinline__ float dpp_add(float v) {
    int t = __builtin_amdgcn_update_dpp(0, __float_as_int(v), CTRL, RM, 0xf, true);
    return v + __int_as_float(t);
}
__device__ __forceinline__ float wave_sum(float v) {
    v = dpp_add<0x111, 0xf>(v); v = dpp_add<0x112, 0xf>(v); v = dpp_add<0x114, 0xf>(v); v = dpp_add<0x118, 0xf>(v);
    v = dpp_add<0x142, 0xa>(v); v = dpp_add<0x143, 0xc>(v);
    return __int_as_float(__builtin_amdgcn_readlane(__float_as_int(v), 63));
}
__device__ __forceinline__ cf wave_sum2(cf v) { return cf{wave_sum(v.x), wave_sum(v.y)}; }

__device__ __forceinline__ void layernorm2(const cf (&v)[8], float act, unsigned (&o)[8]) {
    cf s = ((v[0] + v[1]) + (v[2] + v[3])) + ((v[4] + v[5]) + (v[6] + v[7]));
    cf mean = wave_sum2(s) * (1.0f / DN);
    cf q = cf{0.f, 0.f};
#pragma unroll
    for (int e = 0; e < 8; ++e) { cf d = v[e] - mean; q = cfma(d, d, q); }
    cf var = wave_sum2(q * act) * (1.0f / DN);
    cf m = rbf2(mean);
    cf t = rbf2(rbf2(var) + 1e-5f);
    cf den = rbf2(cf{__builtin_amdgcn_sqrtf(t.x), __builtin_amdgcn_sqrtf(t.y)});
    cf rinv = cf{__builtin_amdgcn_rcpf(den.x), __builtin_amdgcn_rcpf(den.y)};
#pragma unroll
    for (int e = 0; e < 8; ++e) o[e] = pk_bf16(rbf2(v[e] - m) * rinv);
}

__global__ __launch_bounds__(256) void k1_rowfft(const u16* __restrict__ x, cf* __restrict__ T) {
    __shared__ __attribute__((aligned(16))) char smem[4 * 4608];
    const int tid = threadIdx.x, w = tid >> 6, lane = tid & 63, s0 = blockIdx.x;
    char* my = smem + w * 4608;
    const bool act = lane < 56;
    u4 ra = {0, 0, 0, 0}, rb = {0, 0, 0, 0};
    if (act) {
        ra = *(const u4*)(x + (size_t)(s0 + 512 * w) * DN + lane * 8);
        rb = *(const u4*)(x + (size_t)(s0 + 512 * w + 256) * DN + lane * 8);
    }
    Tw tw[8];
    {
        const f4* p = (const f4*)&kTab.w64[lane & 7][0][0];
#pragma unroll
        for (int i = 0; i < 4; ++i) { f4 t = p[i]; tw[2 * i] = mktw(t.xy); tw[2 * i + 1] = mktw(t.zw); }
    }
    cf v[8];
#pragma unroll
    for (int i = 0; i < 4; ++i) {
        v[2 * i] = cf{__uint_as_float(ra[i] << 16), __uint_as_float(rb[i] << 16)};
        v[2 * i + 1] = cf{__uint_as_float(ra[i] & 0xffff0000u), __uint_as_float(rb[i] & 0xffff0000u)};
    }
    unsigned z[8];
    layernorm2(v, act ? 1.f : 0.f, z);
    if (act) {
        *(u4*)(my + lane * 32) = u4{z[0], z[1], z[2], z[3]};
        *(u4*)(my + lane * 32 + 16) = u4{z[4], z[5], z[6], z[7]};
        *(u4*)(my + lane * 32 + 1792) = u4{z[0], z[1], z[2], z[3]};
        *(u4*)(my + lane * 32 + 1792 + 16) = u4{z[4], z[5], z[6], z[7]};
    }
    WSYNC();
    cf y[7];
#pragma unroll
    for (int n1 = 0; n1 < 7; ++n1) y[n1] = unpk_bf16(*(const unsigned*)(my + lane * 28 + n1 * 256));
    dft7<false>(y);
    WSYNC();
#pragma unroll
    for (int k1 = 0; k1 < 7; ++k1) *(cf*)(my + k1 * 576 + lane * 8) = y[k1];
    WSYNC();
    const int k1 = lane >> 3, i8 = lane & 7;
    cf a[8];
#pragma unroll
    for (int b = 0; b < 8; ++b) a[b] = *(const cf*)(my + k1 * 576 + i8 * 8 + b * 64);
    dft8<false>(a);
#pragma unroll
    for (int e = 1; e < 8; ++e) a[e] = cmul(a[e], tw[e]);
    WSYNC();
#pragma unroll
    for (int e = 0; e < 8; ++e) *(cf*)(my + k1 * 576 + e * 72 + i8 * 8) = a[e];
    WSYNC();
#pragma unroll
    for (int n = 0; n < 8; ++n) a[n] = *(const cf*)(my + k1 * 576 + i8 * 72 + n * 8);
    dft8<false>(a);
    WSYNC();
#pragma unroll
    for (int f = 0; f < 8; ++f) *(cf*)(my + k1 * 512 + i8 * 8 + f * 64) = a[f];
    __syncthreads();
    if (tid < DFQ) {
        const int kd = tid, km = (DN - kd) % DN;
        const int ap = (kd % 7) * 512 + (kd & 63) * 8, am = (km % 7) * 512 + (km & 63) * 8;
        cf r[8];
#pragma unroll
        for (int ww = 0; ww < 4; ++ww) {
            cf zp = *(const cf*)(smem + ww * 4608 + ap), zm = *(const cf*)(smem + ww * 4608 + am);
            r[2 * ww] = cfma(zm, cf{1.f, -1.f}, zp);
            r[2 * ww + 1] = cfma(zp.yx, cf{1.f, -1.f}, zm.yx);
        }
        dft8<false>(r);
        const cf* ts = (const cf*)&kTab.ws[s0][0][0];
        cf* o = T + (size_t)s0 * 8 * TROW + (kd == 224 ? 231 : kd);
        o[0] = r[0];
#pragma unroll
        for (int q = 1; q < 8; ++q) o[q * TROW] = cmul(r[q], mktw(ts[q]));
    }
}

__global__ __launch_bounds__(256) void k2_colfft(const cf* __restrict__ T, cf* __restrict__ T2,
                                                 const u16* __restrict__ wr, const u16* __restrict__ wi) {
    __shared__ __attribute__((aligned(16))) char smem[4 * 4608 + 256 * 40];
    const int tid = threadIdx.x, wv = tid >> 6, lane = tid & 63;
    const int q = blockIdx.x & 7, g = blockIdx.x >> 3;
    const int p = tid & 3, r = tid >> 2;
    f4 in[4];
    const size_t goff = ((size_t)r * 8 + q) * TROW + g * 8 + p * 2;
#pragma unroll
    for (int a = 0; a < 4; ++a) in[a] = *(const f4*)(T + goff + (size_t)a * 64 * 8 * TROW);
    const int kd0 = g * 8 > 217 ? 217 : g * 8;
    typedef unsigned u4a __attribute__((ext_vector_type(4), aligned(2)));
    u4 wrv = *(const u4a*)(wr + (size_t)(tid * 8 + q) * DFQ + kd0);
    u4 wiv = *(const u4a*)(wi + (size_t)(tid * 8 + q) * DFQ + kd0);
    Tw t1[4], t2[8];
    {
        const f4* pp = (const f4*)&kTab.w256[r][0][0];
        f4 t = pp[0]; t1[1] = mktw(t.zw);
        t = pp[1]; t1[2] = mktw(t.xy); t1[3] = mktw(t.zw);
        const f4* p2 = (const f4*)&kTab.w64[lane >> 3][0][0];
#pragma unroll
        for (int i = 0; i < 4; ++i) { f4 tt = p2[i]; t2[2 * i] = mktw(tt.xy); t2[2 * i + 1] = mktw(tt.zw); }
    }
    char* wl = smem + 4 * 4608;
    {
        u4 lo, hi;
#pragma unroll
        for (int i = 0; i < 2; ++i) {
            lo[2 * i] = __builtin_amdgcn_perm(wiv[i], wrv[i], 0x05040100u);
            lo[2 * i + 1] = __builtin_amdgcn_perm(wiv[i], wrv[i], 0x07060302u);
            hi[2 * i] = __builtin_amdgcn_perm(wiv[i + 2], wrv[i + 2], 0x05040100u);
            hi[2 * i + 1] = __builtin_amdgcn_perm(wiv[i + 2], wrv[i + 2], 0x07060302u);
        }
        char* dst = wl + tid * 32 + (tid >> 2) * 32;
        *(u4*)dst = lo; *(u4*)(dst + 16) = hi;
    }
    cf c0[4], c1[4];
#pragma unroll
    for (int a = 0; a < 4; ++a) { c0[a] = in[a].xy; c1[a] = in[a].zw; }
    dft4<false>(c0[0], c0[1], c0[2], c0[3]);
    dft4<false>(c1[0], c1[1], c1[2], c1[3]);
#pragma unroll
    for (int b = 1; b < 4; ++b) { c0[b] = cmul(c0[b], t1[b]); c1[b] = cmul(c1[b], t1[b]); }
#pragma unroll
    for (int b = 0; b < 4; ++b) *(f4*)(smem + b * 4608 + tid * 16) = f4{c0[b].x, c0[b].y, c1[b].x, c1[b].y};
    __syncthreads();
    char* my = smem + wv * 4608;
    const int i8 = lane >> 3, c = lane & 7;
    cf a[8];
#pragma unroll
    for (int j = 0; j < 8; ++j) a[j] = *(const cf*)(my + j * 512 + lane * 8);
    dft8<false>(a);
#pragma unroll
    for (int e = 1; e < 8; ++e) a[e] = cmul(a[e], t2[e]);
    WSYNC();
#pragma unroll
    for (int e = 0; e < 8; ++e) *(cf*)(my + e * 576 + lane * 8) = a[e];
    WSYNC();
#pragma unroll
    for (int n = 0; n < 8; ++n) a[n] = *(const cf*)(my + i8 * 576 + n * 64 + c * 8);
    dft8<false>(a);
#pragma unroll
    for (int f = 0; f < 8; ++f) {
        unsigned wp = *(const unsigned*)(wl + wv * 32 + i8 * 160 + f * 1280 + c * 4);
        a[f] = cmul(a[f], mktw(unpk_bf16(wp)));
    }
    dft8<true>(a);
    WSYNC();
#pragma unroll
    for (int n = 0; n < 8; ++n) *(cf*)(my + i8 * 576 + n * 64 + c * 8) = a[n];
    WSYNC();
#pragma unroll
    for (int e = 0; e < 8; ++e) a[e] = *(const cf*)(my + e * 576 + lane * 8);
#pragma unroll
    for (int e = 1; e < 8; ++e) a[e] = cmulc(a[e], t2[e]);
    dft8<true>(a);
    WSYNC();
#pragma unroll
    for (int j = 0; j < 8; ++j) *(cf*)(my + j * 512 + lane * 8) = a[j];
    __syncthreads();
#pragma unroll
    for (int b = 0; b < 4; ++b) { f4 t = *(const f4*)(smem + b * 4608 + tid * 16); c0[b] = t.xy; c1[b] = t.zw; }
#pragma unroll
    for (int b = 1; b < 4; ++b) { c0[b] = cmulc(c0[b], t1[b]); c1[b] = cmulc(c1[b], t1[b]); }
    dft4<true>(c0[0], c0[1], c0[2], c0[3]);
    dft4<true>(c1[0], c1[1], c1[2], c1[3]);
#pragma unroll
    for (int a2 = 0; a2 < 4; ++a2) *(f4*)(T2 + goff + (size_t)a2 * 64 * 8 * TROW) = f4{c0[a2].x, c0[a2].y, c1[a2].x, c1[a2].y};
}

__global__ __launch_bounds__(256) void k3_rowifft(const cf* __restrict__ T2, const u16* __restrict__ x,
                                                  u16* __restrict__ h, u4* __restrict__ Hp) {
    __shared__ __attribute__((aligned(16))) char smem[4 * 4608 + 7168];
    const int tid = threadIdx.x, w = tid >> 6, lane = tid & 63, s0 = blockIdx.x;
    char* my = smem + w * 4608;
    const bool act = lane < 56;
    cf r[8];
    const int kd = tid < DFQ ? tid : 0;
    {
        const cf* src = T2 + (size_t)s0 * 8 * TROW + (kd == 224 ? 231 : kd);
#pragma unroll
        for (int q = 0; q < 8; ++q) r[q] = src[q * TROW];
    }
    u4 ra = {0, 0, 0, 0}, rb = {0, 0, 0, 0};
    if (act) {
        ra = *(const u4*)(x + (size_t)(s0 + 512 * w) * DN + lane * 8);
        rb = *(const u4*)(x + (size_t)(s0 + 512 * w + 256) * DN + lane * 8);
    }
    Tw tw[8];
    {
        const f4* p = (const f4*)&kTab.w64[lane & 7][0][0];
#pragma unroll
        for (int i = 0; i < 4; ++i) { f4 t = p[i]; tw[2 * i] = mktw(t.xy); tw[2 * i + 1] = mktw(t.zw); }
    }
    {
        const float SC = 0.5f / ((float)SN * (float)DN);
        const cf* ts = (const cf*)&kTab.ws[s0][0][0];
        r[0] = r[0] * SC;
#pragma unroll
        for (int q = 1; q < 8; ++q) r[q] = cmulc(r[q], mktw(ts[q] * SC));
        dft8<true>(r);
        const float im = (kd == 0 || kd == 224) ? 0.f : 1.f;
        const int km = (DN - kd) % DN;
        const int ap = (kd % 7) * 576 + (kd & 63) * 8, am = (km % 7) * 576 + (km & 63) * 8;
        if (tid < DFQ) {
#pragma unroll
            for (int ww = 0; ww < 4; ++ww) {
                cf a = r[2 * ww] * cf{1.f, im}, b = r[2 * ww + 1] * cf{1.f, im};
                *(cf*)(smem + ww * 4608 + ap) = addi(a, b);
                *(cf*)(smem + ww * 4608 + am) = cfma(a, cf{1.f, -1.f}, b.yx);
            }
        }
    }
    __syncthreads();
    const int k1 = lane >> 3, i8 = lane & 7;
    cf a[8];
#pragma unroll
    for (int f = 0; f < 8; ++f) a[f] = *(const cf*)(my + k1 * 576 + i8 * 8 + f * 64);
    dft8<true>(a);
#pragma unroll
    for (int n = 1; n < 8; ++n) a[n] = cmulc(a[n], tw[n]);
    WSYNC();
#pragma unroll
    for (int n = 0; n < 8; ++n) *(cf*)(my + k1 * 576 + n * 72 + i8 * 8) = a[n];
    WSYNC();
#pragma unroll
    for (int e = 0; e < 8; ++e) a[e] = *(const cf*)(my + k1 * 576 + i8 * 72 + e * 8);
    dft8<true>(a);
    WSYNC();
#pragma unroll
    for (int b = 0; b < 8; ++b) *(cf*)(my + k1 * 576 + i8 * 8 + b * 64) = a[b];
    WSYNC();
    cf y[7];
#pragma unroll
    for (int k = 0; k < 7; ++k) y[k] = *(const cf*)(my + k * 576 + lane * 8);
    dft7<true>(y);
    WSYNC();
#pragma unroll
    for (int n1 = 0; n1 < 7; ++n1) {
        int n = lane * 7 + n1 * 64; n = n >= DN ? n - DN : n;
        *(unsigned*)(my + n * 4) = pk_bf16(y[n1]);
    }
    WSYNC();
    u4 y0 = *(const u4*)(my + lane * 32), y1 = *(const u4*)(my + lane * 32 + 16);
    unsigned hp[8];
    cf v[8];
    const float actf = act ? 1.f : 0.f;
#pragma unroll
    for (int i = 0; i < 4; ++i) {
        cf xa = cf{__uint_as_float(ra[i] << 16), __uint_as_float(rb[i] << 16)};
        cf xb = cf{__uint_as_float(ra[i] & 0xffff0000u), __uint_as_float(rb[i] & 0xffff0000u)};
        unsigned ya = i < 2 ? y0[2 * i] : y1[2 * i - 4], yb = i < 2 ? y0[2 * i + 1] : y1[2 * i - 3];
        hp[2 * i] = pk_bf16(xa + unpk_bf16(ya));
        hp[2 * i + 1] = pk_bf16(xb + unpk_bf16(yb));
    }
    if (!act) {
#pragma unroll
        for (int e = 0; e < 8; ++e) hp[e] = 0;
    }
#pragma unroll
    for (int e = 0; e < 8; ++e) v[e] = unpk_bf16(hp[e]);
    if (act) {
        u4 ha, hb;
#pragma unroll
        for (int i = 0; i < 4; ++i) {
            ha[i] = __builtin_amdgcn_perm(hp[2 * i + 1], hp[2 * i], 0x05040100u);
            hb[i] = __builtin_amdgcn_perm(hp[2 * i + 1], hp[2 * i], 0x07060302u);
        }
        *(u4*)(h + (size_t)(s0 + 512 * w) * DN + lane * 8) = ha;
        *(u4*)(h + (size_t)(s0 + 512 * w + 256) * DN + lane * 8) = hb;
    }
    unsigned z[8];
    layernorm2(v, actf, z);
#pragma unroll
    for (int e = 0; e < 8; ++e) v[e] = act ? unpk_bf16(z[e]) : cf{0.f, 0.f};
    layernorm2(v, actf, z);
    char* hx = smem + 4 * 4608;
    if (act) {
#pragma unroll
        for (int e = 0; e < 8; ++e) *(unsigned*)(hx + lane * 128 + ((e ^ (lane & 7)) << 4) + w * 4) = z[e];
    }
    __syncthreads();
#pragma unroll
    for (int it = 0; it < 2; ++it) {
        int P = tid + it * 256;
        if (P < DN) {
            int n = P >> 4, rho = P & 15;
            int d = 32 * (n >> 1) + 8 * (rho >> 2) + 4 * (n & 1) + (rho & 3);
            u4 val = *(const u4*)(hx + (d >> 3) * 128 + (((d & 7) ^ ((d >> 3) & 7)) << 4));
            Hp[((size_t)(s0 >> 2) * 28 + n) * 64 + (s0 & 3) * 16 + rho] = val;
        }
    }
}

#define GLDS(gbase, voff, ldsaddr)                                                                        \
    asm volatile("s_mov_b32 m0, %2\n\ts_nop 0\n\tglobal_load_lds_dwordx4 %0, %1" ::"v"(voff), "s"(gbase), "s"(ldsaddr) : "memory")

template <bool G1>
__global__ __launch_bounds__(256) void k_gemm(const u4* __restrict__ Act, const u16* __restrict__ W,
                                              const u16* __restrict__ bias, const u16* __restrict__ h,
                                              void* __restrict__ outp) {
    constexpr int LDW = G1 ? FN : SN;
    constexpr int NK = G1 ? 16 : 14;
    constexpr int PD = 7;
    extern __shared__ __attribute__((aligned(16))) char smem[];
    const int tid = threadIdx.x, lane = tid & 63;
    const int wv = __builtin_amdgcn_readfirstlane(tid >> 6);
    int nb, db;
    if (G1) {
        const int nwg = 196, b = blockIdx.x, xcd = b & 7, off = b >> 3;
        const int qq = nwg / 8, rr = nwg % 8;
        int id = (xcd < rr ? xcd * (qq + 1) : rr * (qq + 1) + (xcd - rr) * qq) + off;
        nb = id / 7; db = id % 7;
    } else {
        const int b = blockIdx.x, xcd = b & 7, off = b >> 3;
        nb = xcd * 4 + off / 7; db = off % 7;
    }
    const int n0 = nb * 64;
    const int t0 = wv * NK;
    const int j = lane >> 3, cs = lane & 7;
    unsigned voff[2];
#pragma unroll
    for (int par = 0; par < 2; ++par) {
        int phi = ((j >> 1) & 1) | (par << 1);
        int c = cs ^ (2 * phi);
        int row = G1 ? 256 * j : 16 * (j >> 2) + (j & 3);
        voff[par] = (unsigned)(row * LDW + n0 + 8 * c) * 2u;
    }
    const unsigned ldsbase = (unsigned)(uintptr_t)smem + wv * 32768;
    const unsigned aoff = (unsigned)lane * 16u;
    const char* Wb = (const char*)W;
    const char* Ab = (const char*)Act + (size_t)db * 4096;
    u4 bfr[NK][4];
    auto issue = [&](int t) {
        const int tg = t0 + t;
#pragma unroll
        for (int i = 0; i < 4; ++i) {
            const char* gb = Wb + (size_t)(G1 ? (4 * tg + i) : (32 * tg + 4 * i)) * (LDW * 2);
            unsigned la = ldsbase + (t & 7) * 4096 + i * 1024;
            GLDS(gb, voff[i & 1], la);
        }
        const char* ab = Ab + (size_t)tg * (28 * 1024);
#pragma unroll
        for (int n = 0; n < 4; ++n)
            asm volatile("global_load_dwordx4 %0, %1, %2 offset:%3" : "=v"(bfr[t][n]) : "v"(aoff), "s"(ab), "i"(n * 1024) : "memory");
    };
#pragma unroll
    for (int t = 0; t < PD; ++t) issue(t);
    unsigned traddr[4];
    {
        const int g = lane >> 4, q = (lane >> 2) & 3, p = lane & 3;
        const int phi = ((q >> 1) & 1) | ((g & 1) << 1);
#pragma unroll
        for (int m = 0; m < 4; ++m) traddr[m] = ldsbase + (8 * g + q) * 128 + ((m ^ phi) * 32) + p * 8;
    }
    f4 acc[4][4];
#pragma unroll
    for (int m = 0; m < 4; ++m)
#pragma unroll
        for (int n = 0; n < 4; ++n) acc[m][n] = f4{0.f, 0.f, 0.f, 0.f};
#pragma unroll
    for (int t = 0; t < NK; ++t) {
        const int rem = (NK - t < PD ? NK - t : PD) - 1;
        asm volatile("s_waitcnt vmcnt(%4)" : "+v"(bfr[t][0]), "+v"(bfr[t][1]), "+v"(bfr[t][2]), "+v"(bfr[t][3]) : "i"(rem * 8) : "memory");
        u2 wf[4][2];
#pragma unroll
        for (int m = 0; m < 4; ++m)
#pragma unroll
            for (int hh = 0; hh < 2; ++hh)
                asm volatile("ds_read_b64_tr_b16 %0, %1 offset:%2" : "=v"(wf[m][hh]) : "v"(traddr[m]), "i"((t & 7) * 4096 + hh * 512) : "memory");
        if (t + PD < NK) issue(t + PD);
        asm volatile("s_waitcnt lgkmcnt(0)" : "+v"(wf[0][0]), "+v"(wf[0][1]), "+v"(wf[1][0]), "+v"(wf[1][1]), "+v"(wf[2][0]), "+v"(wf[2][1]), "+v"(wf[3][0]), "+v"(wf[3][1]) :: "memory");
#pragma unroll
        for (int m = 0; m < 4; ++m) {
            bf8 wfrag = __builtin_bit_cast(bf8, u4{wf[m][0].x, wf[m][0].y, wf[m][1].x, wf[m][1].y});
#pragma unroll
            for (int n = 0; n < 4; ++n) {
                bf8 afrag = __builtin_bit_cast(bf8, bfr[t][n]);
                if (G1) acc[m][n] = __builtin_amdgcn_mfma_f32_16x16x32_bf16(wfrag, afrag, acc[m][n], 0, 0, 0);
                else acc[m][n] = __builtin_amdgcn_mfma_f32_16x16x32_bf16(afrag, wfrag, acc[m][n], 0, 0, 0);
            }
        }
    }
    __syncthreads();
#pragma unroll
    for (int m = 0; m < 4; ++m)
#pragma unroll
        for (int n = 0; n < 4; ++n)
            *(f4*)(smem + wv * 16384 + (m * 4 + n) * 1024 + lane * 16) = acc[m][n];
    __syncthreads();
    const int mb = 2 * (wv >> 1), nbb = 2 * (wv & 1);
    f4 res[2][2];
#pragma unroll
    for (int mi = 0; mi < 2; ++mi)
#pragma unroll
        for (int ni = 0; ni < 2; ++ni) {
            const char* p = smem + ((mb + mi) * 4 + nbb + ni) * 1024 + lane * 16;
            res[mi][ni] = (*(const f4*)p + *(const f4*)(p + 16384)) + (*(const f4*)(p + 32768) + *(const f4*)(p + 49152));
        }
    const int g = lane >> 4, col = lane & 15;
    if (G1) {
        u4* Up = (u4*)outp;
        u2 bv[2];
#pragma unroll
        for (int mi = 0; mi < 2; ++mi) bv[mi] = *(const u2*)(bias + n0 + 16 * (mb + mi) + 4 * g);
#pragma unroll
        for (int ni = 0; ni < 2; ++ni) {
            u4 o;
#pragma unroll
            for (int mi = 0; mi < 2; ++mi)
#pragma unroll
                for (int hh = 0; hh < 2; ++hh) {
                    cf a = hh ? res[mi][ni].zw : res[mi][ni].xy;
                    cf vv = rbf2(rbf2(a) + unpk_bf16(bv[mi][hh]));
                    cf zz = vv * cfma(vv * vv, cf{0.044715f, 0.044715f}, cf{1.f, 1.f}) * (-2.0f * 0.7978845608028654f * 1.4426950408889634f);
                    cf ee = cf{__builtin_amdgcn_exp2f(zz.x), __builtin_amdgcn_exp2f(zz.y)} + 1.0f;
                    cf gg = vv * cf{__builtin_amdgcn_rcpf(ee.x), __builtin_amdgcn_rcpf(ee.y)};
                    o[mi * 2 + hh] = pk_bf16(gg);
                }
            const int ks = nb * 2 + (wv >> 1);
            Up[((size_t)ks * 28 + db * 4 + nbb + ni) * 64 + lane] = o;
        }
    } else {
        u16* out = (u16*)outp;
#pragma unroll
        for (int mi = 0; mi < 2; ++mi) {
            const int s = n0 + 16 * (mb + mi) + col;
            const int d = db * 64 + 32 * (wv & 1) + 8 * g;
            u4 hv = *(const u4*)(h + (size_t)s * DN + d);
            float bb = __uint_as_float(((unsigned)bias[s]) << 16);
            u4 o;
#pragma unroll
            for (int ni = 0; ni < 2; ++ni)
#pragma unroll
                for (int hh = 0; hh < 2; ++hh) {
                    cf a = hh ? res[mi][ni].zw : res[mi][ni].xy;
                    cf vv = rbf2(rbf2(a) + bb);
                    o[ni * 2 + hh] = pk_bf16(unpk_bf16(hv[ni * 2 + hh]) + vv);
                }
            *(u4*)(out + (size_t)s * DN + d) = o;
        }
    }
}

extern "C" void kernel_launch(void* const* d_in, const int* in_sizes, int n_in,
                              void* d_out, int out_size, void* d_ws, size_t ws_size,
                              hipStream_t stream) {
    const u16* x = (const u16*)d_in[0];
    const u16* wr = (const u16*)d_in[1];
    const u16* wi = (const u16*)d_in[2];
    const u16* w1 = (const u16*)d_in[4];
    const u16* b1 = (const u16*)d_in[5];
    const u16* w2 = (const u16*)d_in[6];
    const u16* b2 = (const u16*)d_in[7];
    char* ws = (char*)d_ws;
    cf* T = (cf*)ws;
    cf* T2 = (cf*)(ws + (4 << 20));
    u16* h = (u16*)(ws + (8 << 20));
    u4* Hp = (u4*)(ws + (10 << 20));
    u4* Up = (u4*)(ws + (12 << 20));
    (void)hipFuncSetAttribute((const void*)k_gemm<true>, hipFuncAttributeMaxDynamicSharedMemorySize, 131072);
    (void)hipFuncSetAttribute((const void*)k_gemm<false>, hipFuncAttributeMaxDynamicSharedMemorySize, 131072);
    k1_rowfft<<<256, 256, 0, stream>>>(x, T);
    k2_colfft<<<232, 256, 0, stream>>>(T, T2, wr, wi);
    k3_rowifft<<<256, 256, 0, stream>>>(T2, x, h, Hp);
    k_gemm<true><<<196, 256, 131072, stream>>>(Hp, w1, b1, h, Up);
    k_gemm<false><<<224, 256, 131072, stream>>>(Up, w2, b2, h, d_out);
}
```

```cpp
#include <hip/hip_runtime.h>
#include <hip/hip_bf16.h>
#include <stdint.h>

typedef unsigned short u16;
typedef float cf __attribute__((ext_vector_type(2)));
typedef float f4 __attribute__((ext_vector_type(4)));
typedef unsigned u4 __attribute__((ext_vector_type(4)));
typedef unsigned u2 __attribute__((ext_vector_type(2)));
typedef __bf16 bf2 __attribute__((ext_vector_type(2)));
typedef __bf16 bf8 __attribute__((ext_vector_type(8)));

#define SN 2048
#define DN 448
#define DFQ 225
#define FN 1792

constexpr double cx_pi = 3.14159265358979323846264338327950288;
constexpr double cx_sin_t(double x) { double x2 = x * x, t = x, s = x; for (int i = 1; i < 12; ++i) { t *= -x2 / ((2 * i) * (2 * i + 1)); s += t; } return s; }
constexpr double cx_cos_t(double x) { double x2 = x * x, t = 1, s = 1; for (int i = 1; i < 12; ++i) { t *= -x2 / ((2 * i - 1) * (2 * i)); s += t; } return s; }
constexpr double cx_c2048(int n) {
    n &= 2047; int q = n >> 9, r = n & 511; double a = 2.0 * cx_pi * r / 2048.0;
    double c = 0, s = 0;
    if (r <= 256) { c = cx_cos_t(a); s = cx_sin_t(a); } else { double b = 2.0 * cx_pi * (512 - r) / 2048.0; c = cx_sin_t(b); s = cx_cos_t(b); }
    return q == 0 ? c : q == 1 ? -s : q == 2 ? -c : s;
}
constexpr double cx_s2048(int n) { return cx_c2048(n - 512); }
struct Tables {
    float ws[256][8][2];
    float w64[8][8][2];
    float w256[64][4][2];
};
struct CosTab { float c[2048]; };
constexpr CosTab make_cos() { CosTab t{}; for (int i = 0; i < 2048; ++i) t.c[i] = (float)cx_c2048(i); return t; }
constexpr CosTab kCos = make_cos();
constexpr Tables make_tables() {
    Tables t{};
    for (int s = 0; s < 256; ++s) for (int q = 0; q < 8; ++q) { int n = (s * q) & 2047; t.ws[s][q][0] = kCos.c[n]; t.ws[s][q][1] = -kCos.c[(n - 512) & 2047]; }
    for (int i = 0; i < 8; ++i) for (int e = 0; e < 8; ++e) { int n = (i * e * 32) & 2047; t.w64[i][e][0] = kCos.c[n]; t.w64[i][e][1] = -kCos.c[(n - 512) & 2047]; }
    for (int r = 0; r < 64; ++r) for (int b = 0; b < 4; ++b) { int n = (r * b * 8) & 2047; t.w256[r][b][0] = kCos.c[n]; t.w256[r][b][1] = -kCos.c[(n - 512) & 2047]; }
    return t;
}
__device__ const __attribute__((aligned(64))) Tables kTab = make_tables();

#define STAMP_DECL
#define STAMP(i) do {} while (0)
#define STAMP_FLUSH(k) do {} while (0)
#define WSYNC() do { __builtin_amdgcn_fence(__ATOMIC_ACQ_REL, "wavefront"); __builtin_amdgcn_wave_barrier(); } while (0)
__device__ __forceinline__ cf cfma(cf a, cf b, cf c) { return __builtin_elementwise_fma(a, b, c); }
__device__ __forceinline__ cf addi(cf a, cf b) { return cfma(b.yx, cf{-1.f, 1.f}, a); }
__device__ __forceinline__ cf subi(cf a, cf b) { return cfma(b.yx, cf{1.f, -1.f}, a); }
struct Tw { cf w; cf n; };
__device__ __forceinline__ Tw mktw(cf w) { Tw t; t.w = w; t.n = cf{-w.y, w.y}; return t; }
__device__ __forceinline__ cf cmul(cf a, const Tw& t) { return cfma(a.yx, t.n, a * t.w.xx); }
__device__ __forceinline__ cf cmulc(cf a, const Tw& t) { return cfma(a.yx, t.n.yx, a * t.w.xx); }

template <bool INV> __device__ __forceinline__ cf rot_m(cf a, cf b) { return INV ? addi(a, b) : subi(a, b); }
template <bool INV> __device__ __forceinline__ cf rot_p(cf a, cf b) { return INV ? subi(a, b) : addi(a, b); }

template <bool INV> __device__ __forceinline__ void dft8(cf (&x)[8]) {
    const float R = 0.70710678118654752440f;
    cf a0 = x[0] + x[4], a4 = x[0] - x[4], a1 = x[1] + x[5], a5 = x[1] - x[5];
    cf a2 = x[2] + x[6], a6 = x[2] - x[6], a3 = x[3] + x[7], a7 = x[3] - x[7];
    cf b0 = a0 + a2, b2 = a0 - a2, b1 = a1 + a3, b3 = a1 - a3;
    cf c0 = rot_m<INV>(a4, a6), c2 = rot_p<INV>(a4, a6);
    cf e = rot_m<INV>(a5, a7), f = rot_p<INV>(a5, a7);
    cf g = rot_m<INV>(e, e), h = rot_m<INV>(f, f);
    x[0] = b0 + b1; x[4] = b0 - b1;
    x[2] = rot_m<INV>(b2, b3); x[6] = rot_p<INV>(b2, b3);
    x[1] = cfma(g, cf{R, R}, c0); x[5] = cfma(g, cf{-R, -R}, c0);
    x[3] = cfma(h.yx, INV ? cf{-R, R} : cf{R, -R}, c2);
    x[7] = cfma(h.yx, INV ? cf{R, -R} : cf{-R, R}, c2);
}
template <bool INV> __device__ __forceinline__ void dft4(cf& x0, cf& x1, cf& x2, cf& x3) {
    cf a0 = x0 + x2, a2 = x0 - x2, a1 = x1 + x3, a3 = x1 - x3;
    x0 = a0 + a1; x2 = a0 - a1; x1 = rot_m<INV>(a2, a3); x3 = rot_p<INV>(a2, a3);
}
template <bool INV> __device__ __forceinline__ void dft7(cf (&x)[7]) {
    const float C1 = 0.62348980185873353053f, C2 = -0.22252093395631440429f, C3 = -0.90096886790241912624f;
    const float S1 = 0.78183148246802980871f, S2 = 0.97492791218182360702f, S3 = 0.43388373911755812048f;
    cf s1 = x[1] + x[6], d1 = x[1] - x[6], s2 = x[2] + x[5], d2 = x[2] - x[5], s3 = x[3] + x[4], d3 = x[3] - x[4];
    cf x0 = x[0];
    cf A1 = cfma(s3, cf{C3, C3}, cfma(s2, cf{C2, C2}, cfma(s1, cf{C1, C1}, x0)));
    cf A2 = cfma(s3, cf{C1, C1}, cfma(s2, cf{C3, C3}, cfma(s1, cf{C2, C2}, x0)));
    cf A3 = cfma(s3, cf{C2, C2}, cfma(s2, cf{C1, C1}, cfma(s1, cf{C3, C3}, x0)));
    cf B1 = cfma(d3, cf{S3, S3}, cfma(d2, cf{S2, S2}, d1 * cf{S1, S1}));
    cf B2 = cfma(d3, cf{-S1, -S1}, cfma(d2, cf{-S3, -S3}, d1 * cf{S2, S2}));
    cf B3 = cfma(d3, cf{S2, S2}, cfma(d2, cf{-S1, -S1}, d1 * cf{S3, S3}));
    x[0] = (x0 + s1) + (s2 + s3);
    x[1] = rot_m<INV>(A1, B1); x[6] = rot_p<INV>(A1, B1);
    x[2] = rot_m<INV>(A2, B2); x[5] = rot_p<INV>(A2, B2);
    x[3] = rot_m<INV>(A3, B3); x[4] = rot_p<INV>(A3, B3);
}

__device__ __forceinline__ unsigned pk_bf16(cf v) { return __builtin_bit_cast(unsigned, __builtin_convertvector(v, bf2)); }
__device__ __forceinline__ cf unpk_bf16(unsigned p) { return cf{__uint_as_float(p << 16), __uint_as_float(p & 0xffff0000u)}; }
__device__ __forceinline__ cf rbf2(cf v) { return unpk_bf16(pk_bf16(v)); }

template <int CTRL, int RM> __device__ __forceinline__ float dpp_add(float v) {
    int t = __builtin_amdgcn_update_dpp(0, __float_as_int(v), CTRL, RM, 0xf, true);
    return v + __int_as_float(t);
}
__device__ __forceinline__ float swap32_add(float a, float b) {
    auto r = __builtin_amdgcn_permlane32_swap(__float_as_uint(a), __float_as_uint(b), false, false);
    return __uint_as_float(r[0]) + __uint_as_float(r[1]);
}
__device__ __forceinline__ float swap16_add(float a, float b) {
    auto r = __builtin_amdgcn_permlane16_swap(__float_as_uint(a), __float_as_uint(b), false, false);
    return __uint_as_float(r[0]) + __uint_as_float(r[1]);
}
__device__ __forceinline__ float rdlane(float v, int l) { return __int_as_float(__builtin_amdgcn_readlane(__float_as_int(v), l)); }
__device__ __forceinline__ void wave_sum4(cf& s, cf& q) {
    float v = swap16_add(swap32_add(s.x, s.y), swap32_add(q.x, q.y));
    v = dpp_add<0x111, 0xf>(v); v = dpp_add<0x112, 0xf>(v); v = dpp_add<0x114, 0xf>(v); v = dpp_add<0x118, 0xf>(v);
    s = cf{rdlane(v, 15), rdlane(v, 47)};
    q = cf{rdlane(v, 31), rdlane(v, 63)};
}

__device__ __forceinline__ void layernorm2(const cf (&v)[8], float act, unsigned (&o)[8]) {
    const cf piv = cf{rdlane(v[0].x, 0), rdlane(v[0].y, 0)};
    cf d[8];
#pragma unroll
    for (int e = 0; e < 8; ++e) d[e] = v[e] - piv;
    cf s = ((d[0] + d[1]) + (d[2] + d[3])) + ((d[4] + d[5]) + (d[6] + d[7]));
    cf q0 = cfma(d[2], d[2], d[0] * d[0]), q1 = cfma(d[3], d[3], d[1] * d[1]);
    q0 = cfma(d[4], d[4], q0); q1 = cfma(d[5], d[5], q1);
    q0 = cfma(d[6], d[6], q0); q1 = cfma(d[7], d[7], q1);
    cf q = (q0 + q1) * act;
    s = s * act;
    wave_sum4(s, q);
    const cf md = s * (1.0f / DN);
    cf var = cfma(-md, md, q * (1.0f / DN));
    var = cf{__builtin_fmaxf(var.x, 0.f), __builtin_fmaxf(var.y, 0.f)};
    const cf m = rbf2(piv + md);
    const cf t = rbf2(rbf2(var) + 1e-5f);
    const cf den = rbf2(cf{__builtin_amdgcn_sqrtf(t.x), __builtin_amdgcn_sqrtf(t.y)});
    const cf rinv = cf{__builtin_amdgcn_rcpf(den.x), __builtin_amdgcn_rcpf(den.y)};
#pragma unroll
    for (int e = 0; e < 8; ++e) o[e] = pk_bf16(rbf2(v[e] - m) * rinv);
}

#define WS_FLAGS 0
#define WS_FLAG_BYTES (3 * 8 * 4096)
#define WS_T   (1 << 20)
#define WS_T2  (5 << 20)
#define WS_H   (9 << 20)
#define WS_HP  (11 << 20)
#define WS_UP  (13 << 20)
#define WS_END (15 << 20)

#define GAS __attribute__((address_space(1)))
#define RLX_AGENT __ATOMIC_RELAXED, __HIP_MEMORY_SCOPE_AGENT
#define PF_LOAD(dst, base, off) asm volatile("global_load_dword %0, %1, %2" : "=v"(dst) : "v"(off), "s"(base) : "memory")
__device__ __forceinline__ int s0_of_block(int b) { return (b & ~15) | ((b & 7) << 1) | ((b >> 3) & 1); }

#define PFSLEEP 8
#define PFW2 1
__global__ __launch_bounds__(320) void k1_rowfft(const u16* __restrict__ x, char* __restrict__ ws,
                                                 const char* __restrict__ wr, const char* __restrict__ wi,
                                                 const char* __restrict__ w1, const char* __restrict__ w2) {
    __shared__ __attribute__((aligned(16))) char smem[4 * 4608];
    const int tid = threadIdx.x, lane = tid & 63, b = blockIdx.x, s0 = s0_of_block(b);
    const int w = __builtin_amdgcn_readfirstlane(tid >> 6);
    if (w == 4) {
        if (lane < WS_FLAG_BYTES / 256 / 16) *(u4*)(ws + WS_FLAGS + b * (WS_FLAG_BYTES / 256) + lane * 16) = u4{0, 0, 0, 0};
        __builtin_amdgcn_s_sleep(PFSLEEP);
        const int xcd = b & 7, i = b >> 3, piece = lane & 7, rl = lane >> 3;
        unsigned d[20];
#pragma unroll
        for (int j = 0; j < 20; ++j) d[j] = 0;
        unsigned off = (unsigned)(((8 * i + rl) * 8 + xcd) * 450 + (piece * 128 < 446 ? piece * 128 : 446));
        PF_LOAD(d[0], wr, off); PF_LOAD(d[1], wi, off);
        unsigned o1 = (unsigned)(b * 28672 + lane * 128);
#pragma unroll
        for (int j = 0; j < 4; ++j) { unsigned o = o1 + (j < 3 ? j * 8192 : 20480); PF_LOAD(d[2 + j], w1, o); }
#pragma unroll
        for (int j = 0; j < 4; ++j) { unsigned o = o1 + (j < 3 ? j * 8192 : 20480); PF_LOAD(d[10 + j], w2, o); }
        asm volatile("s_waitcnt vmcnt(0)" : "+v"(d[0]), "+v"(d[1]), "+v"(d[2]), "+v"(d[3]), "+v"(d[4]), "+v"(d[5]), "+v"(d[6]), "+v"(d[7]),
                     "+v"(d[8]), "+v"(d[9]), "+v"(d[10]), "+v"(d[11]), "+v"(d[12]), "+v"(d[13]) :: "memory");
        return;
    }
    STAMP_DECL;
    STAMP(0);
    char* my = smem + w * 4608;
    const int lc = lane < 56 ? lane : 55;
    const u4 ra = *(const u4*)(x + (size_t)(s0 + 512 * w) * DN + lc * 8);
    const u4 rb = *(const u4*)(x + (size_t)(s0 + 512 * w + 256) * DN + lc * 8);
    Tw tw[8], ts[8];
    {
        const f4* p = (const f4*)&kTab.w64[lane & 7][0][0];
#pragma unroll
        for (int i = 0; i < 4; ++i) { f4 t = p[i]; tw[2 * i] = mktw(t.xy); tw[2 * i + 1] = mktw(t.zw); }
        const cf* tp = (const cf*)&kTab.ws[s0][0][0];
#pragma unroll
        for (int q = 1; q < 8; ++q) ts[q] = mktw(tp[q]);
    }
    cf v[8];
#pragma unroll
    for (int i = 0; i < 4; ++i) {
        v[2 * i] = cf{__uint_as_float(ra[i] << 16), __uint_as_float(rb[i] << 16)};
        v[2 * i + 1] = cf{__uint_as_float(ra[i] & 0xffff0000u), __uint_as_float(rb[i] & 0xffff0000u)};
    }
    STAMP(1);
    unsigned z[8];
    layernorm2(v, lane < 56 ? 1.f : 0.f, z);
    *(u4*)(my + lane * 32) = u4{z[0], z[1], z[2], z[3]};
    *(u4*)(my + lane * 32 + 16) = u4{z[4], z[5], z[6], z[7]};
    WSYNC();
    *(u4*)(my + lane * 32 + 1792) = u4{z[0], z[1], z[2], z[3]};
    *(u4*)(my + lane * 32 + 1792 + 16) = u4{z[4], z[5], z[6], z[7]};
    WSYNC();
    cf y[7];
#pragma unroll
    for (int n1 = 0; n1 < 7; ++n1) y[n1] = unpk_bf16(*(const unsigned*)(my + lane * 28 + n1 * 256));
    dft7<false>(y);
    WSYNC();
#pragma unroll
    for (int k1 = 0; k1 < 7; ++k1) *(cf*)(my + k1 * 576 + lane * 8) = y[k1];
    WSYNC();
    const int k1 = lane >> 3, i8 = lane & 7;
    cf a[8];
#pragma unroll
    for (int bb = 0; bb < 8; ++bb) a[bb] = *(const cf*)(my + k1 * 576 + i8 * 8 + bb * 64);
    dft8<false>(a);
#pragma unroll
    for (int e = 1; e < 8; ++e) a[e] = cmul(a[e], tw[e]);
    WSYNC();
#pragma unroll
    for (int e = 0; e < 8; ++e) *(cf*)(my + k1 * 576 + e * 72 + i8 * 8) = a[e];
    WSYNC();
#pragma unroll
    for (int n = 0; n < 8; ++n) a[n] = *(const cf*)(my + k1 * 576 + i8 * 72 + n * 8);
    dft8<false>(a);
    WSYNC();
#pragma unroll
    for (int f = 0; f < 8; ++f) *(cf*)(my + k1 * 512 + i8 * 8 + f * 64) = a[f];
    __syncthreads();
    {
        const int kd = tid, km = kd == 0 ? 0 : DN - kd;
        const int ap = (kd % 7) * 512 + (kd & 63) * 8, am = (km % 7) * 512 + (km & 63) * 8;
        cf r[8];
#pragma unroll
        for (int ww = 0; ww < 4; ++ww) {
            cf zp = *(const cf*)(smem + ww * 4608 + ap), zm = *(const cf*)(smem + ww * 4608 + am);
            r[2 * ww] = cfma(zm, cf{1.f, -1.f}, zp);
            r[2 * ww + 1] = cfma(zp.yx, cf{1.f, -1.f}, zm.yx);
        }
        dft8<false>(r);
        const int pos = kd < 224 ? kd : kd == 224 ? 231 : 224 + (kd % 7);
        char* o = ws + WS_T + ((pos >> 3) * 256 + s0) * 64 + (pos & 7) * 8;
        *(cf*)o = r[0];
#pragma unroll
        for (int q = 1; q < 8; ++q) *(cf*)(o + q * (29 * 16384)) = cmul(r[q], ts[q]);
    }
    STAMP(6);
    STAMP_FLUSH(0);
}

#define ACT_SC " sc1"
#define NA 4
#define GPD 4
#define LDS_SCR 131072
#define LDS_TOTAL 163840
#define LD16(dst, voff, base) asm volatile("global_load_dwordx4 %0, %1, %2" : "=v"(dst) : "v"(voff), "s"(base) : "memory")
#define LD16O(dst, voff, base, imm) asm volatile("global_load_dwordx4 %0, %1, %2 offset:%3" : "=v"(dst) : "v"(voff), "s"(base), "i"(imm) : "memory")
#define DRAIN() asm volatile("s_waitcnt vmcnt(0)" ::: "memory")

__device__ __forceinline__ u4 make_srd(const char* base, unsigned bytes) {
    return u4{(unsigned)(uintptr_t)base, (unsigned)((uintptr_t)base >> 32) & 0xffffu, bytes, 0x00020000u};
}
template <bool G1, int T0, int N>
__device__ __forceinline__ void stage_w(u4 srd, unsigned voff0, unsigned voff1, unsigned ldsbase) {
    constexpr int LDW = G1 ? FN : SN;
#pragma unroll
    for (int t = 0; t < N; ++t)
#pragma unroll
        for (int i = 0; i < 4; ++i) {
            const int goff = (G1 ? (4 * (T0 + t) + i) : (32 * (T0 + t) + 4 * i)) * (LDW * 2);
            unsigned tmp;
            asm volatile("s_add_i32 m0, %2, %4\n\ts_mov_b32 %0, %5\n\tbuffer_load_dwordx4 %3, %1, %0 offen lds"
                         : "=&s"(tmp) : "s"(srd), "s"(ldsbase), "v"((i & 1) ? voff1 : voff0), "i"(t * 4096 + i * 1024), "i"(goff) : "memory", "scc");
        }
}
template <int A0, int N>
__device__ __forceinline__ void move_w(const unsigned (&traddr)[4]) {
#pragma unroll
    for (int t = 0; t < N; ++t)
#pragma unroll
        for (int m = 0; m < 4; ++m)
#pragma unroll
            for (int hh = 0; hh < 2; ++hh)
                asm volatile("ds_read_b64_tr_b16 a[%c1:%c2], %0 offset:%c3" ::"v"(traddr[m]), "i"(16 * (A0 + t) + 4 * m + 2 * hh),
                             "i"(16 * (A0 + t) + 4 * m + 2 * hh + 1), "i"(t * 4096 + hh * 512) : "memory");
    asm volatile("s_waitcnt lgkmcnt(0)" ::: "memory");
}
__device__ __forceinline__ void publish(char* ws, int seam, int idx, int tid) {
    if (tid < 8) __hip_atomic_store((GAS unsigned*)(ws + WS_FLAGS + (seam * 8 + tid) * 4096 + idx * 16), 1u, RLX_AGENT);
}
__device__ __forceinline__ void poll(char* ws, int seam, int rep, int idx) {
    GAS unsigned* p = (GAS unsigned*)(ws + WS_FLAGS + (seam * 8 + rep) * 4096 + idx * 16);
    for (int spin = 0; spin < (1 << 16); ++spin) {
        unsigned v = __hip_atomic_load(p, RLX_AGENT);
        if (__all(v != 0u)) break;
    }
    __builtin_amdgcn_fence(__ATOMIC_ACQUIRE, "wavefront");
}

template <bool G1, int NK, int PD, typename F>
__device__ __forceinline__ void gemm_loop(f4 (&acc)[4][4], const char* abase, unsigned aoff0, unsigned aoff1, F&& after_issue) {
    u4 bfr[NK][4];
    auto issue1 = [&](int t, int n) {
        const char* ab = abase + (size_t)t * (28 * 1024);
        asm volatile("global_load_dwordx4 %0, %1, %2 offset:%3" ACT_SC : "=v"(bfr[t][n]) : "v"(n < 2 ? aoff0 : aoff1), "s"(ab), "i"((n & 1) * 1024) : "memory");
    };
#pragma unroll
    for (int t = 0; t < PD; ++t)
#pragma unroll
        for (int n = 0; n < 4; ++n) issue1(t, n);
    after_issue();
#pragma unroll
    for (int t = 0; t < NK; ++t) {
        const int last = (t + PD - 1 < NK - 1) ? t + PD - 1 : NK - 1;
        asm volatile("s_waitcnt vmcnt(%4)" : "+v"(bfr[t][0]), "+v"(bfr[t][1]), "+v"(bfr[t][2]), "+v"(bfr[t][3]) : "i"((last - t) * 4) : "memory");
#pragma unroll
        for (int n = 0; n < 4; ++n) {
#pragma unroll
            for (int m = 0; m < 4; ++m) {
                if (t == 0) {
                    if (G1) asm volatile("v_mfma_f32_16x16x32_bf16 %0, a[%c2:%c3], %1, 0" : "=&v"(acc[m][n]) : "v"(bfr[t][n]), "i"(16 * t + 4 * m), "i"(16 * t + 4 * m + 3));
                    else asm volatile("v_mfma_f32_16x16x32_bf16 %0, %1, a[%c2:%c3], 0" : "=&v"(acc[m][n]) : "v"(bfr[t][n]), "i"(16 * t + 4 * m), "i"(16 * t + 4 * m + 3));
                } else {
                    if (G1) asm volatile("v_mfma_f32_16x16x32_bf16 %0, a[%c2:%c3], %1, %0" : "+v"(acc[m][n]) : "v"(bfr[t][n]), "i"(16 * t + 4 * m), "i"(16 * t + 4 * m + 3));
                    else asm volatile("v_mfma_f32_16x16x32_bf16 %0, %1, a[%c2:%c3], %0" : "+v"(acc[m][n]) : "v"(bfr[t][n]), "i"(16 * t + 4 * m), "i"(16 * t + 4 * m + 3));
                }
                if (m == 1 && t + PD < NK) issue1(t + PD, n);
            }
        }
    }
    asm volatile("s_nop 7\n\ts_nop 1" : "+v"(acc[0][0]), "+v"(acc[0][1]), "+v"(acc[0][2]), "+v"(acc[0][3]), "+v"(acc[1][0]), "+v"(acc[1][1]), "+v"(acc[1][2]), "+v"(acc[1][3]),
                 "+v"(acc[2][0]), "+v"(acc[2][1]), "+v"(acc[2][2]), "+v"(acc[2][3]), "+v"(acc[3][0]), "+v"(acc[3][1]), "+v"(acc[3][2]), "+v"(acc[3][3]));
}
__device__ __forceinline__ void reduce4(f4 (&acc)[4][4], char* smem, int wv, int lane) {
#pragma unroll
    for (int c = 1; c < 4; ++c) {
        char* dst = (c < 3 ? smem + LDS_SCR + (wv * 2 + c - 1) * 4096 : smem + wv * 32768 + 7 * 4096) + lane * 16;
#pragma unroll
        for (int mi = 0; mi < 2; ++mi)
#pragma unroll
            for (int ni = 0; ni < 2; ++ni) *(f4*)(dst + (mi * 2 + ni) * 1024) = acc[2 * (c >> 1) + mi][2 * (c & 1) + ni];
    }
    __syncthreads();
#pragma unroll
    for (int c = 1; c < 4; ++c) {
        const int sw = wv ^ c;
        const char* src = (c < 3 ? smem + LDS_SCR + (sw * 2 + c - 1) * 4096 : smem + sw * 32768 + 7 * 4096) + lane * 16;
#pragma unroll
        for (int mi = 0; mi < 2; ++mi)
#pragma unroll
            for (int ni = 0; ni < 2; ++ni) acc[mi][ni] += *(const f4*)(src + (mi * 2 + ni) * 1024);
    }
}

__global__ __launch_bounds__(256, 1) void kf_fused(char* __restrict__ ws, const char* __restrict__ x,
                                                   const char* __restrict__ wr, const char* __restrict__ wi,
                                                   const char* __restrict__ w1, const char* __restrict__ b1,
                                                   const char* __restrict__ w2, const char* __restrict__ b2,
                                                   u16* __restrict__ out) {
    extern __shared__ __attribute__((aligned(16))) char smem[];
    asm volatile("" ::: "a0", "a255");
    const int tid = threadIdx.x, lane = tid & 63, b = blockIdx.x;
    const int wv = __builtin_amdgcn_readfirstlane(tid >> 6);
    const int xcd = b & 7, off = b >> 3;
    STAMP_DECL;
    STAMP(0);
    const auto rs = __builtin_amdgcn_make_buffer_rsrc(ws, 0, WS_END, 0x00020000);
    char* scr = smem + LDS_SCR;
    const bool doS2 = b < 232;
    const int q = xcd, g = doS2 ? off : 0;
    const int s0 = s0_of_block(b);
    const int db = (xcd & 1) ? 4 + off % 3 : (off & 3);
    const int nl = (xcd & 1) ? off / 3 : off >> 2;
    const bool has1 = nl < 7, has2 = nl < 8;
    const int nb1 = 7 * (xcd >> 1) + (has1 ? nl : 0), nb2 = 8 * (xcd >> 1) + (has2 ? nl : 0);
    const int p = tid & 3, r = tid >> 2;
    f4 in[4], t1r[2], t2r[4], twr[4];
    u4 wrv, wiv, ra, rb;
    const char* t64 = (const char*)&kTab.w64[0][0][0];
    {
        const char* tb = ws + WS_T + (q * 29 + g) * 16384;
        unsigned o = tid * 16;
        LD16(in[0], o, tb); LD16(in[1], o, tb + 4096); LD16(in[2], o, tb + 8192); LD16(in[3], o, tb + 12288);
        const char* t256 = (const char*)&kTab.w256[0][0][0];
        unsigned o1 = r * 32;
        LD16O(t1r[0], o1, t256, 0); LD16O(t1r[1], o1, t256, 16);
        const int kd0 = g * 8 > 217 ? 217 : g * 8;
        unsigned ow = (unsigned)((tid * 8 + q) * DFQ + kd0) * 2u;
        LD16(wrv, ow, wr); LD16(wiv, ow, wi);
        unsigned o2 = (lane >> 3) * 64;
        LD16O(t2r[0], o2, t64, 0); LD16O(t2r[1], o2, t64, 16); LD16O(t2r[2], o2, t64, 32); LD16O(t2r[3], o2, t64, 48);
    }
    const int lc = lane < 56 ? lane : 55;
    const unsigned xoff = (unsigned)((s0 + 512 * wv) * DN + lc * 8) * 2u;
    const int gq = lane >> 4, col = lane & 15;
    const int mb = 2 * (wv >> 1);
    const unsigned ldsbase = (unsigned)(uintptr_t)smem + wv * 32768;
    unsigned v1o[2], v2o[2], traddr[4];
    {
        const int j = lane >> 3, cs = lane & 7;
#pragma unroll
        for (int par = 0; par < 2; ++par) {
            const int c = cs ^ (2 * (((j >> 1) & 1) | (par << 1)));
            v1o[par] = (unsigned)(256 * j * FN + nb1 * 64 + 8 * c) * 2u;
            v2o[par] = (unsigned)((16 * (j >> 2) + (j & 3)) * SN + nb2 * 64 + 8 * c) * 2u;
        }
        const int qq = (lane >> 2) & 3, pp = lane & 3;
        const int phi = ((qq >> 1) & 1) | ((gq & 1) << 1);
#pragma unroll
        for (int m = 0; m < 4; ++m) traddr[m] = ldsbase + (8 * gq + qq) * 128 + (((m ^ mb) ^ phi) * 32) + pp * 8;
    }
    const u4 w1w = make_srd(w1 + (size_t)wv * (64 * FN * 2), (unsigned)(SN * FN * 2 - wv * (64 * FN * 2)));
    const u4 w2w = make_srd(w2 + (size_t)wv * (448 * SN * 2), (unsigned)(SN * FN * 2 - wv * (448 * SN * 2)));
    STAMP(8);
    stage_w<true, 0, NA>(w1w, v1o[0], v1o[1], ldsbase);
    STAMP(9);
    asm volatile("s_waitcnt vmcnt(%12)" : "+v"(in[0]), "+v"(in[1]), "+v"(in[2]), "+v"(in[3]), "+v"(t1r[0]), "+v"(t1r[1]), "+v"(wrv), "+v"(wiv),
                 "+v"(t2r[0]), "+v"(t2r[1]), "+v"(t2r[2]), "+v"(t2r[3]) : "i"(NA * 4) : "memory");
    STAMP(1);
    {
        Tw t1[4], t2[8];
        t1[1] = mktw(t1r[0].zw); t1[2] = mktw(t1r[1].xy); t1[3] = mktw(t1r[1].zw);
#pragma unroll
        for (int i = 0; i < 4; ++i) { t2[2 * i] = mktw(t2r[i].xy); t2[2 * i + 1] = mktw(t2r[i].zw); }
        cf c0[4], c1[4];
#pragma unroll
        for (int a = 0; a < 4; ++a) { c0[a] = in[a].xy; c1[a] = in[a].zw; }
        dft4<false>(c0[0], c0[1], c0[2], c0[3]);
        dft4<false>(c1[0], c1[1], c1[2], c1[3]);
#pragma unroll
        for (int bb = 1; bb < 4; ++bb) { c0[bb] = cmul(c0[bb], t1[bb]); c1[bb] = cmul(c1[bb], t1[bb]); }
#pragma unroll
        for (int bb = 0; bb < 4; ++bb) *(f4*)(scr + bb * 4608 + tid * 16) = f4{c0[bb].x, c0[bb].y, c1[bb].x, c1[bb].y};
        char* wl = scr + 4 * 4608;
        {
            u4 lo, hi;
#pragma unroll
            for (int i = 0; i < 2; ++i) {
                lo[2 * i] = __builtin_amdgcn_perm(wiv[i], wrv[i], 0x05040100u);
                lo[2 * i + 1] = __builtin_amdgcn_perm(wiv[i], wrv[i], 0x07060302u);
                hi[2 * i] = __builtin_amdgcn_perm(wiv[i + 2], wrv[i + 2], 0x05040100u);
                hi[2 * i + 1] = __builtin_amdgcn_perm(wiv[i + 2], wrv[i + 2], 0x07060302u);
            }
            char* dst = wl + tid * 32 + (tid >> 2) * 32;
            *(u4*)dst = lo; *(u4*)(dst + 16) = hi;
        }
        __syncthreads();
        char* my = scr + wv * 4608;
        const int i8 = lane >> 3, c = lane & 7;
        cf a[8];
#pragma unroll
        for (int j = 0; j < 8; ++j) a[j] = *(const cf*)(my + j * 512 + lane * 8);
        unsigned wp[8];
#pragma unroll
        for (int f = 0; f < 8; ++f) wp[f] = *(const unsigned*)(wl + wv * 32 + i8 * 160 + f * 1280 + c * 4);
        dft8<false>(a);
#pragma unroll
        for (int e = 1; e < 8; ++e) a[e] = cmul(a[e], t2[e]);
        WSYNC();
#pragma unroll
        for (int e = 0; e < 8; ++e) *(cf*)(my + e * 576 + lane * 8) = a[e];
        WSYNC();
#pragma unroll
        for (int n = 0; n < 8; ++n) a[n] = *(const cf*)(my + i8 * 576 + n * 64 + c * 8);
        dft8<false>(a);
#pragma unroll
        for (int f = 0; f < 8; ++f) a[f] = cmul(a[f], mktw(unpk_bf16(wp[f])));
        dft8<true>(a);
        WSYNC();
#pragma unroll
        for (int n = 0; n < 8; ++n) *(cf*)(my + i8 * 576 + n * 64 + c * 8) = a[n];
        WSYNC();
#pragma unroll
        for (int e = 0; e < 8; ++e) a[e] = *(const cf*)(my + e * 576 + lane * 8);
#pragma unroll
        for (int e = 1; e < 8; ++e) a[e] = cmulc(a[e], t2[e]);
        dft8<true>(a);
        WSYNC();
#pragma unroll
        for (int j = 0; j < 8; ++j) *(cf*)(my + j * 512 + lane * 8) = a[j];
        __syncthreads();
#pragma unroll
        for (int bb = 0; bb < 4; ++bb) { f4 t = *(const f4*)(scr + bb * 4608 + tid * 16); c0[bb] = t.xy; c1[bb] = t.zw; }
#pragma unroll
        for (int bb = 1; bb < 4; ++bb) { c0[bb] = cmulc(c0[bb], t1[bb]); c1[bb] = cmulc(c1[bb], t1[bb]); }
        dft4<true>(c0[0], c0[1], c0[2], c0[3]);
        dft4<true>(c1[0], c1[1], c1[2], c1[3]);
        STAMP(10);
        DRAIN();
        if (doS2) {
#pragma unroll
            for (int a2 = 0; a2 < 4; ++a2)
                __builtin_amdgcn_raw_buffer_store_b128(__builtin_bit_cast(u4, f4{c0[a2].x, c0[a2].y, c1[a2].x, c1[a2].y}), rs,
                                                       tid * 16 + a2 * 4096 + WS_T2 + (q * 29 + g) * 16384, 0, 16);
        }
    }
    STAMP(2);
    move_w<0, NA>(traddr);
    DRAIN();
    __syncthreads();
    STAMP(11);
    if (doS2) publish(ws, 0, b, tid);
    u2 bv[2]; unsigned bs[2];
    {
        unsigned o3 = (lane & 7) * 64;
        LD16O(twr[0], o3, t64, 0); LD16O(twr[1], o3, t64, 16); LD16O(twr[2], o3, t64, 32); LD16O(twr[3], o3, t64, 48);
        LD16(ra, xoff, x); LD16(rb, xoff, x + 256 * DN * 2);
        unsigned o = (unsigned)(nb1 * 64 + 16 * mb + 4 * gq) * 2u, ob = (unsigned)(nb2 * 64 + 16 * mb + col) * 2u;
        asm volatile("global_load_dwordx2 %0, %1, %2" : "=v"(bv[0]) : "v"(o), "s"(b1) : "memory");
        asm volatile("global_load_dwordx2 %0, %1, %2 offset:32" : "=v"(bv[1]) : "v"(o), "s"(b1) : "memory");
        asm volatile("global_load_ushort %0, %1, %2" : "=v"(bs[0]) : "v"(ob), "s"(b2) : "memory");
        asm volatile("global_load_ushort %0, %1, %2 offset:32" : "=v"(bs[1]) : "v"(ob), "s"(b2) : "memory");
    }
    cf tsr[8];
    {
        const cf* tp = (const cf*)&kTab.ws[s0][0][0];
#pragma unroll
        for (int i = 1; i < 8; ++i) tsr[i] = tp[i];
    }
    stage_w<true, NA, 8>(w1w, v1o[0], v1o[1], ldsbase);
    poll(ws, 0, xcd, tid < 232 ? tid : 0);
    asm volatile("" : "+v"(twr[0]), "+v"(twr[1]), "+v"(twr[2]), "+v"(twr[3]), "+v"(ra), "+v"(rb), "+v"(bv[0]), "+v"(bv[1]), "+v"(bs[0]), "+v"(bs[1]) :: "memory");
    __syncthreads();
    STAMP(3);
    {
        char* my = scr + wv * 4608;
        const int kd = tid;
        cf rr[8];
        {
            const int pos = kd < 224 ? kd : 231;
            const unsigned vo = ((pos >> 3) * 256 + s0) * 64 + (pos & 7) * 8;
#pragma unroll
            for (int qq = 0; qq < 8; ++qq)
                asm volatile("global_load_dwordx2 %0, %1, %2 sc1" : "=v"(rr[qq]) : "v"(vo), "s"(ws + WS_T2 + qq * (29 * 16384)) : "memory");
        }
        move_w<NA, 8>(traddr);
        stage_w<true, NA + 8, 8 - NA>(w1w, v1o[0], v1o[1], ldsbase);
        asm volatile("s_waitcnt vmcnt(%8)" : "+v"(rr[0]), "+v"(rr[1]), "+v"(rr[2]), "+v"(rr[3]), "+v"(rr[4]), "+v"(rr[5]), "+v"(rr[6]), "+v"(rr[7]) : "i"((8 - NA) * 4) : "memory");
        Tw tw[8];
#pragma unroll
        for (int i = 0; i < 4; ++i) { tw[2 * i] = mktw(twr[i].xy); tw[2 * i + 1] = mktw(twr[i].zw); }
#pragma unroll
        for (int qq = 1; qq < 8; ++qq) rr[qq] = cmulc(rr[qq], mktw(tsr[qq]));
        dft8<true>(rr);
        STAMP(12);
        const float SC = 0.5f / ((float)SN * (float)DN);
        const float im = (kd == 0 || kd == 224) ? 0.f : SC;
        const int km = kd == 0 ? 0 : DN - kd;
        int ap = (kd % 7) * 576 + (kd & 63) * 8, am = (km % 7) * 576 + (km & 63) * 8;
        if (kd >= DFQ) { ap = 7 * 576 + lane * 8; am = ap; }
#pragma unroll
        for (int ww = 0; ww < 4; ++ww) {
            cf a = rr[2 * ww] * cf{SC, im}, bb = rr[2 * ww + 1] * cf{SC, im};
            *(cf*)(scr + ww * 4608 + ap) = addi(a, bb);
            *(cf*)(scr + ww * 4608 + am) = cfma(a, cf{1.f, -1.f}, bb.yx);
        }
        __syncthreads();
        const int k1 = lane >> 3, i8 = lane & 7;
        cf a[8];
#pragma unroll
        for (int f = 0; f < 8; ++f) a[f] = *(const cf*)(my + k1 * 576 + i8 * 8 + f * 64);
        dft8<true>(a);
#pragma unroll
        for (int n = 1; n < 8; ++n) a[n] = cmulc(a[n], tw[n]);
        WSYNC();
#pragma unroll
        for (int n = 0; n < 8; ++n) *(cf*)(my + k1 * 576 + n * 72 + i8 * 8) = a[n];
        WSYNC();
#pragma unroll
        for (int e = 0; e < 8; ++e) a[e] = *(const cf*)(my + k1 * 576 + i8 * 72 + e * 8);
        dft8<true>(a);
        WSYNC();
#pragma unroll
        for (int bb = 0; bb < 8; ++bb) *(cf*)(my + k1 * 576 + i8 * 8 + bb * 64) = a[bb];
        WSYNC();
        cf y[7];
#pragma unroll
        for (int k = 0; k < 7; ++k) y[k] = *(const cf*)(my + k * 576 + lane * 8);
        dft7<true>(y);
        WSYNC();
#pragma unroll
        for (int n1 = 0; n1 < 7; ++n1) {
            int n = lane * 7 + n1 * 64; n = n >= DN ? n - DN : n;
            *(unsigned*)(my + n * 4) = pk_bf16(y[n1]);
        }
        WSYNC();
        u4 y0 = *(const u4*)(my + lc * 32), y1 = *(const u4*)(my + lc * 32 + 16);
        unsigned hp[8];
        cf v[8];
        const float actf = lane < 56 ? 1.f : 0.f;
#pragma unroll
        for (int i = 0; i < 4; ++i) {
            cf xa = cf{__uint_as_float(ra[i] << 16), __uint_as_float(rb[i] << 16)};
            cf xb = cf{__uint_as_float(ra[i] & 0xffff0000u), __uint_as_float(rb[i] & 0xffff0000u)};
            unsigned ya = i < 2 ? y0[2 * i] : y1[2 * i - 4], yb = i < 2 ? y0[2 * i + 1] : y1[2 * i - 3];
            hp[2 * i] = pk_bf16(xa + unpk_bf16(ya));
            hp[2 * i + 1] = pk_bf16(xb + unpk_bf16(yb));
        }
#pragma unroll
        for (int e = 0; e < 8; ++e) v[e] = unpk_bf16(hp[e]);
        {
            u4 ha, hb;
#pragma unroll
            for (int i = 0; i < 4; ++i) {
                ha[i] = __builtin_amdgcn_perm(hp[2 * i + 1], hp[2 * i], 0x05040100u);
                hb[i] = __builtin_amdgcn_perm(hp[2 * i + 1], hp[2 * i], 0x07060302u);
            }
            __builtin_amdgcn_raw_buffer_store_b128(ha, rs, xoff + WS_H, 0, 16);
            __builtin_amdgcn_raw_buffer_store_b128(hb, rs, xoff + (WS_H + 256 * DN * 2), 0, 16);
        }
        unsigned z[8];
        layernorm2(v, actf, z);
#pragma unroll
        for (int e = 0; e < 8; ++e) v[e] = unpk_bf16(z[e]);
        layernorm2(v, actf, z);
        char* hx = scr + 4 * 4608;
#pragma unroll
        for (int e = 0; e < 8; ++e) *(unsigned*)(hx + lane * 128 + ((e ^ (lane & 7)) << 4) + wv * 4) = z[e];
        __syncthreads();
#pragma unroll
        for (int it = 0; it < 2; ++it) {
            if (it == 0 || wv < 3) {
                const int P = tid + it * 256;
                const int n = P >> 4, rho = P & 15;
                const int d = 32 * (n >> 1) + 8 * (rho >> 2) + 4 * (n & 1) + (rho & 3);
                u4 val = *(const u4*)(hx + (d >> 3) * 128 + (((d & 7) ^ ((d >> 3) & 7)) << 4));
                __builtin_amdgcn_raw_buffer_store_b128(val, rs, n * 1024 + rho * 16 + WS_HP + (s0 >> 2) * (28 * 1024) + (s0 & 3) * 256, 0, 16);
            }
        }
    }
    STAMP(4);
    asm volatile("s_waitcnt vmcnt(3)" ::: "memory");
    move_w<NA + 8, 8 - NA>(traddr);
    DRAIN();
    __syncthreads();
    STAMP(13);
    publish(ws, 1, s0, tid);
    stage_w<false, 0, 7>(w2w, v2o[0], v2o[1], ldsbase);
    const unsigned aoff0 = (unsigned)lane * 16u + (wv & 1) * 2048u, aoff1 = (unsigned)lane * 16u + (1 - (wv & 1)) * 2048u;
    f4 acc[4][4];
    if (has1) {
        poll(ws, 1, xcd, tid);
        STAMP(14);
        gemm_loop<true, 16, GPD>(acc, ws + WS_HP + db * 4096 + (size_t)wv * (16 * 28 * 1024), aoff0, aoff1, [] {});
        STAMP(5);
        reduce4(acc, smem, wv, lane);
#pragma unroll
        for (int ni = 0; ni < 2; ++ni) {
            u4 o;
#pragma unroll
            for (int mi = 0; mi < 2; ++mi)
#pragma unroll
                for (int hh = 0; hh < 2; ++hh) {
                    cf a = hh ? acc[mi][ni].zw : acc[mi][ni].xy;
                    cf vv = rbf2(rbf2(a) + unpk_bf16(bv[mi][hh]));
                    cf zz = vv * cfma(vv * vv, cf{0.044715f, 0.044715f}, cf{1.f, 1.f}) * (-2.0f * 0.7978845608028654f * 1.4426950408889634f);
                    cf ee = cf{__builtin_amdgcn_exp2f(zz.x), __builtin_amdgcn_exp2f(zz.y)} + 1.0f;
                    cf gg = vv * cf{__builtin_amdgcn_rcpf(ee.x), __builtin_amdgcn_rcpf(ee.y)};
                    o[mi * 2 + hh] = pk_bf16(gg);
                }
            const int ks = nb1 * 2 + (wv >> 1);
            __builtin_amdgcn_raw_buffer_store_b128(o, rs, lane * 16 + ni * 1024 + WS_UP + (ks * 28 + db * 4 + 2 * (wv & 1)) * 1024, 0, 16);
        }
    } else {
        DRAIN();
    }
    if (!has2) return;
    move_w<0, 7>(traddr);
    DRAIN();
    __syncthreads();
    STAMP(15);
    if (has1) publish(ws, 2, db * 28 + nb1, tid);
    stage_w<false, 7, 7>(w2w, v2o[0], v2o[1], ldsbase);
    poll(ws, 2, xcd, db * 28 + wv * 7 + (lane < 7 ? lane : 0));
    STAMP(6);
    u4 hv[2];
    const unsigned ho = (unsigned)((nb2 * 64 + 16 * mb + col) * DN + db * 64 + 32 * (wv & 1) + 8 * gq) * 2u;
    asm volatile("global_load_dwordx4 %0, %1, %2 sc1" : "=v"(hv[0]) : "v"(ho), "s"(ws + WS_H) : "memory");
    asm volatile("global_load_dwordx4 %0, %1, %2 sc1" : "=v"(hv[1]) : "v"(ho), "s"(ws + WS_H + 16 * DN * 2) : "memory");
    gemm_loop<false, 14, GPD>(acc, ws + WS_UP + db * 4096 + (size_t)wv * (14 * 28 * 1024), aoff0, aoff1, [&] { move_w<7, 7>(traddr); });
    asm volatile("" : "+v"(hv[0]), "+v"(hv[1]));
    reduce4(acc, smem, wv, lane);
#pragma unroll
    for (int mi = 0; mi < 2; ++mi) {
        const float bb = __uint_as_float(bs[mi] << 16);
        u4 o;
#pragma unroll
        for (int ni = 0; ni < 2; ++ni)
#pragma unroll
            for (int hh = 0; hh < 2; ++hh) {
                cf a = hh ? acc[mi][ni].zw : acc[mi][ni].xy;
                cf vv = rbf2(rbf2(a) + bb);
                o[ni * 2 + hh] = pk_bf16(unpk_bf16(hv[mi][ni * 2 + hh]) + vv);
            }
        *(u4*)((char*)out + ho + mi * (16 * DN * 2)) = o;
    }
    STAMP_FLUSH(2);
}

extern "C" void kernel_launch(void* const* d_in, const int* in_sizes, int n_in,
                              void* d_out, int out_size, void* d_ws, size_t ws_size,
                              hipStream_t stream) {
    const char* x = (const char*)d_in[0];
    const char* wr = (const char*)d_in[1];
    const char* wi = (const char*)d_in[2];
    const char* w1 = (const char*)d_in[4];
    const char* b1 = (const char*)d_in[5];
    const char* w2 = (const char*)d_in[6];
    const char* b2 = (const char*)d_in[7];
    char* ws = (char*)d_ws;
    (void)hipFuncSetAttribute((const void*)kf_fused, hipFuncAttributeMaxDynamicSharedMemorySize, LDS_TOTAL);
    k1_rowfft<<<256, 320, 0, stream>>>((const u16*)x, ws, wr, wi, w1, w2);
    kf_fused<<<256, 256, LDS_TOTAL, stream>>>(ws, x, wr, wi, w1, b1, w2, b2, (u16*)d_out);
}
```

```cpp
#include <hip/hip_runtime.h>
#include <hip/hip_bf16.h>
#include <stdint.h>

typedef unsigned short u16;
typedef float cf __attribute__((ext_vector_type(2)));
typedef float f4 __attribute__((ext_vector_type(4)));
typedef unsigned u4 __attribute__((ext_vector_type(4)));
typedef unsigned u2 __attribute__((ext_vector_type(2)));
typedef __bf16 bf2 __attribute__((ext_vector_type(2)));
typedef __bf16 bf8 __attribute__((ext_vector_type(8)));
typedef _Float16 h2 __attribute__((ext_vector_type(2)));
typedef _Float16 h8 __attribute__((ext_vector_type(8)));

#define SN 2048
#define DN 448
#define DFQ 225
#define FN 1792

#define STAMP_DECL
#define STAMP(i) do {} while (0)
#define STAMP_FLUSH(k) do {} while (0)
#define WSYNC() do { __builtin_amdgcn_fence(__ATOMIC_ACQ_REL, "wavefront"); __builtin_amdgcn_wave_barrier(); } while (0)
__device__ __forceinline__ cf cfma(cf a, cf b, cf c) { return __builtin_elementwise_fma(a, b, c); }
__device__ __forceinline__ cf addi(cf a, cf b) { return cfma(b.yx, cf{-1.f, 1.f}, a); }
__device__ __forceinline__ cf subi(cf a, cf b) { return cfma(b.yx, cf{1.f, -1.f}, a); }
struct Tw { cf w; cf n; };
__device__ __forceinline__ Tw mktw(cf w) { Tw t; t.w = w; t.n = cf{-w.y, w.y}; return t; }
__device__ __forceinline__ cf cmul(cf a, const Tw& t) { return cfma(a.yx, t.n, a * t.w.xx); }
__device__ __forceinline__ cf cmulc(cf a, const Tw& t) { return cfma(a.yx, t.n.yx, a * t.w.xx); }

template <bool INV> __device__ __forceinline__ cf rot_m(cf a, cf b) { return INV ? addi(a, b) : subi(a, b); }
template <bool INV> __device__ __forceinline__ cf rot_p(cf a, cf b) { return INV ? subi(a, b) : addi(a, b); }

template <bool INV> __device__ __forceinline__ void dft8(cf (&x)[8]) {
    const float R = 0.70710678118654752440f;
    cf a0 = x[0] + x[4], a4 = x[0] - x[4], a1 = x[1] + x[5], a5 = x[1] - x[5];
    cf a2 = x[2] + x[6], a6 = x[2] - x[6], a3 = x[3] + x[7], a7 = x[3] - x[7];
    cf b0 = a0 + a2, b2 = a0 - a2, b1 = a1 + a3, b3 = a1 - a3;
    cf c0 = rot_m<INV>(a4, a6), c2 = rot_p<INV>(a4, a6);
    cf e = rot_m<INV>(a5, a7), f = rot_p<INV>(a5, a7);
    cf g = rot_m<INV>(e, e), h = rot_m<INV>(f, f);
    x[0] = b0 + b1; x[4] = b0 - b1;
    x[2] = rot_m<INV>(b2, b3); x[6] = rot_p<INV>(b2, b3);
    x[1] = cfma(g, cf{R, R}, c0); x[5] = cfma(g, cf{-R, -R}, c0);
    x[3] = cfma(h.yx, INV ? cf{-R, R} : cf{R, -R}, c2);
    x[7] = cfma(h.yx, INV ? cf{R, -R} : cf{-R, R}, c2);
}
__device__ __forceinline__ unsigned pk_f16(cf v) { return __builtin_bit_cast(unsigned, __builtin_convertvector(v, h2)); }
__device__ __forceinline__ cf unpk_f16(unsigned p) { return __builtin_convertvector(__builtin_bit_cast(h2, p), cf); }
__device__ __forceinline__ unsigned pk_bf16(cf v) { return __builtin_bit_cast(unsigned, __builtin_convertvector(v, bf2)); }
__device__ __forceinline__ cf unpk_bf16(unsigned p) { return cf{__uint_as_float(p << 16), __uint_as_float(p & 0xffff0000u)}; }
__device__ __forceinline__ cf rbf2(cf v) { return unpk_bf16(pk_bf16(v)); }

template <int CTRL, int RM> __device__ __forceinline__ float dpp_add(float v) {
    int t = __builtin_amdgcn_update_dpp(0, __float_as_int(v), CTRL, RM, 0xf, true);
    return v + __int_as_float(t);
}
__device__ __forceinline__ float swap32_add(float a, float b) {
    auto r = __builtin_amdgcn_permlane32_swap(__float_as_uint(a), __float_as_uint(b), false, false);
    return __uint_as_float(r[0]) + __uint_as_float(r[1]);
}
__device__ __forceinline__ float swap16_add(float a, float b) {
    auto r = __builtin_amdgcn_permlane16_swap(__float_as_uint(a), __float_as_uint(b), false, false);
    return __uint_as_float(r[0]) + __uint_as_float(r[1]);
}
__device__ __forceinline__ float rdlane(float v, int l) { return __int_as_float(__builtin_amdgcn_readlane(__float_as_int(v), l)); }
__device__ __forceinline__ void wave_sum4(cf& s, cf& q) {
    float v = swap16_add(swap32_add(s.x, s.y), swap32_add(q.x, q.y));
    v = dpp_add<0x111, 0xf>(v); v = dpp_add<0x112, 0xf>(v); v = dpp_add<0x114, 0xf>(v); v = dpp_add<0x118, 0xf>(v);
    s = cf{rdlane(v, 15), rdlane(v, 47)};
    q = cf{rdlane(v, 31), rdlane(v, 63)};
}

template <int NE, bool MASK>
__device__ __forceinline__ void row_stats(const cf (&v)[NE], float act, cf& mean, cf& var) {
    const cf piv = cf{rdlane(v[0].x, 0), rdlane(v[0].y, 0)};
    cf d[NE];
#pragma unroll
    for (int e = 0; e < NE; ++e) d[e] = v[e] - piv;
    cf s = ((d[0] + d[1]) + (d[2] + d[3])) + ((d[4] + d[5]) + (NE == 8 ? d[6] + d[NE - 1] : d[6]));
    cf q0 = cfma(d[2], d[2], d[0] * d[0]), q1 = cfma(d[3], d[3], d[1] * d[1]);
    q0 = cfma(d[4], d[4], q0); q1 = cfma(d[5], d[5], q1);
    q0 = cfma(d[6], d[6], q0);
    if (NE == 8) q1 = cfma(d[NE - 1], d[NE - 1], q1);
    cf q = q0 + q1;
    if (MASK) { q = q * act; s = s * act; }
    wave_sum4(s, q);
    const cf md = s * (1.0f / DN);
    var = cfma(-md, md, q * (1.0f / DN));
    var = cf{__builtin_fmaxf(var.x, 0.f), __builtin_fmaxf(var.y, 0.f)};
    mean = piv + md;
}
__device__ __forceinline__ cf rsq2(cf v) { return cf{__builtin_amdgcn_rsqf(v.x), __builtin_amdgcn_rsqf(v.y)}; }
__device__ __forceinline__ float cosr(float rev) { return __builtin_amdgcn_cosf(rev); }
__device__ __forceinline__ float sinr(float rev) { return __builtin_amdgcn_sinf(rev); }
#define PIN(x) asm volatile("" : "+v"(x))
template <int CTRL, int RM> __device__ __forceinline__ unsigned dpp_max(unsigned v) {
    unsigned t = (unsigned)__builtin_amdgcn_update_dpp(0, (int)v, CTRL, RM, 0xf, true);
    return v > t ? v : t;
}
__device__ __forceinline__ unsigned wave_max(unsigned v) {
    v = dpp_max<0x111, 0xf>(v); v = dpp_max<0x112, 0xf>(v); v = dpp_max<0x114, 0xf>(v); v = dpp_max<0x118, 0xf>(v);
    v = dpp_max<0x142, 0xa>(v); v = dpp_max<0x143, 0xc>(v);
    return (unsigned)__builtin_amdgcn_readlane((int)v, 63);
}

#define WS_FLAGS 0
#define WS_FLAG_BYTES (3 * 8 * 4096)
#define WS_WMAX (128 << 10)
#define WS_T   (1 << 20)
#define WS_T2  (5 << 20)
#define WS_H   (7 << 20)
#define WS_HP  (9 << 20)
#define WS_UP  (11 << 20)
#define WS_END (13 << 20)

#define GAS __attribute__((address_space(1)))
#define RLX_AGENT __ATOMIC_RELAXED, __HIP_MEMORY_SCOPE_AGENT
#define PF_LOAD(dst, base, off) asm volatile("global_load_dword %0, %1, %2" : "=v"(dst) : "v"(off), "s"(base) : "memory")
__device__ __forceinline__ int s0_of_block(int b) { return (b & ~31) | ((b & 7) << 2) | ((b >> 3) & 3); }
__device__ __forceinline__ f4 mfma16(u4 a, u4 b, f4 c) {
    return __builtin_amdgcn_mfma_f32_16x16x32_f16(__builtin_bit_cast(h8, a), __builtin_bit_cast(h8, b), c, 0, 0, 0);
}
__device__ __forceinline__ u4 cvt8(f4 a, f4 b) { return u4{pk_f16(a.xy), pk_f16(a.zw), pk_f16(b.xy), pk_f16(b.zw)}; }

__device__ __forceinline__ u4 make_srd(const char* base, unsigned bytes) {
    return u4{(unsigned)(uintptr_t)base, (unsigned)((uintptr_t)base >> 32) & 0xffffu, bytes, 0x00020000u};
}
#define GLDS_B(srd, voff, soff, ldsaddr) asm volatile("s_mov_b32 m0, %3\n\ts_nop 0\n\tbuffer_load_dwordx4 %0, %1, %2 offen lds" :: "v"(voff), "s"(srd), "s"(soff), "s"(ldsaddr) : "memory")
#define CODE_ADDR(dst, sym) do { unsigned long long pc_; int dl_; \
    asm volatile("s_getpc_b64 %0\n.Lpc_%=:\n\ts_mov_b32 %1, " sym "-.Lpc_%=" : "=s"(pc_), "=s"(dl_)); dst = (const char*)(pc_ + (long long)dl_); } while (0)
#define K1_CODE_LINES 40
#define PFW2 1
extern "C" __global__ __launch_bounds__(256) __attribute__((amdgpu_waves_per_eu(1, 2))) void k1_rowfft(const u16* __restrict__ x, char* __restrict__ ws,
                                                 const char* __restrict__ wr, const char* __restrict__ wi,
                                                 const char* __restrict__ w1, const char* __restrict__ w2) {
    __shared__ __attribute__((aligned(16))) char smem[21504 + 16];
    const int tid = threadIdx.x, lane = tid & 63, b = blockIdx.x & 255, s0 = s0_of_block(b);
    const int w = __builtin_amdgcn_readfirstlane(tid >> 6);
    const int k1 = lane >> 4, ka = 16 * w + (lane & 15);
    const unsigned tw_off = (unsigned)(WS_T + ((8 * k1 + (ka >> 3)) * 256 + s0) * 64 + ((ka >> 1) & 3) * 16 + (ka & 1) * 4);
    if (blockIdx.x >= 256) {
        const unsigned lb = (unsigned)(uintptr_t)smem;
        {
            const u4 srd = make_srd((w & 1) ? wi : wr, SN * DFQ * 2);
            const int blk = (w >> 1) ? 255 - s0 : s0;
            const unsigned vo = lane * 16;
#pragma unroll
            for (int i = 0; i < 4; ++i) GLDS_B(srd, vo, (unsigned)(blk * 3600 + i * 1024), lb + w * 5120 + i * 1024);
            GLDS_B(srd, vo, (unsigned)(((blk + 1) & 255) * 3600), lb + w * 5120 + 4096);
        }
        unsigned d0;
        {
            const int cl = (b >> 3) * 8 + (lane & 7);
            unsigned clen;
            asm volatile("s_mov_b32 %0, .Lkf_code_end-kf_fused" : "=s"(clen));
            const unsigned o3 = (unsigned)cl * 128u < clen ? (unsigned)cl * 128u : 0u;
            const char* code; CODE_ADDR(code, "kf_fused");
            PF_LOAD(d0, code, o3);
            const u4 s1 = make_srd(w1, SN * FN * 2), s2 = make_srd(w2, SN * FN * 2);
            const unsigned vo = lane * 16 + b * 28672 + w * 7168;
#pragma unroll
            for (int i = 0; i < 7; ++i) GLDS_B(s1, vo, (unsigned)(i * 1024), lb + 20480);
#pragma unroll
            for (int i = 0; i < 7; ++i) GLDS_B(s2, vo, (unsigned)(i * 1024), lb + 20480);
        }
        int kd = 64 * k1 + 7 * ka; kd = kd >= DN ? kd - DN : kd;
        const bool mir = kd > 224;
        const int col = mir ? DN - kd : kd;
        asm volatile("s_waitcnt vmcnt(%0)" :: "i"(8 + 7 * PFW2) : "memory");
        __builtin_amdgcn_s_barrier();
        asm volatile("" ::: "memory");
        unsigned vr[8], vi[8];
#pragma unroll
        for (int q = 0; q < 8; ++q) {
            const int o = col * 2 + (mir ? 10240 + (q == 0 ? 4096 : (8 - q) * 450) : q * 450);
            vr[q] = *(const u16*)(smem + o); vi[q] = *(const u16*)(smem + o + 5120);
        }
        if (tid < WS_FLAG_BYTES / 256 / 16) *(u4*)(ws + WS_FLAGS + b * (WS_FLAG_BYTES / 256) + tid * 16) = u4{0, 0, 0, 0};
        const unsigned sg = mir ? 0x80000000u : 0u;
        unsigned mx = 0;
#pragma unroll
        for (int q = 0; q < 8; ++q) {
            *(unsigned*)(ws + tw_off + 8 + q * (32 * 16384)) = (vr[q] | (vi[q] << 16)) ^ sg;
            const unsigned a = vr[q] & 0x7fffu, c = vi[q] & 0x7fffu;
            mx = mx > a ? mx : a; mx = mx > c ? mx : c;
        }
        mx = wave_max(mx);
        if (lane == 0) *(unsigned*)(smem + 21504 + w * 4) = mx;
        __syncthreads();
        if (tid == 0) {
            const u4 m4 = *(const u4*)(smem + 21504);
            const unsigned m01 = m4.x > m4.y ? m4.x : m4.y, m23 = m4.z > m4.w ? m4.z : m4.w;
            *(unsigned*)(ws + WS_WMAX + b * 4) = m01 > m23 ? m01 : m23;
        }
        asm volatile("s_waitcnt vmcnt(0)" : "+v"(d0) :: "memory");
        return;
    }
    STAMP_DECL;
    STAMP(0);
    cf v[7];
    unsigned pfc;
    {
        const char* code; CODE_ADDR(code, "k1_rowfft");
        const unsigned oc = (lane < K1_CODE_LINES ? lane : 0) * 128;
        PF_LOAD(pfc, code, oc);
    }
    {
        const char* xa = (const char*)x + (size_t)(s0 + 512 * w) * (DN * 2);
        const unsigned o = lane * 2;
#pragma unroll
        for (int e = 0; e < 7; ++e) {
            float a = 0.f, c = 0.f;
            asm volatile("global_load_short_d16_hi %0, %1, %2 offset:%3" : "+v"(a) : "v"(o), "s"(xa), "i"(128 * e) : "memory");
            asm volatile("global_load_short_d16_hi %0, %1, %2 offset:%3" : "+v"(c) : "v"(o), "s"(xa + 256 * DN * 2), "i"(128 * e) : "memory");
            v[e] = cf{a, c};
        }
        __builtin_amdgcn_sched_barrier(0);
    }
    u4 cb[2][2], ca[4];
#pragma unroll
    for (int ks = 0; ks < 2; ++ks)
#pragma unroll
        for (int jp = 0; jp < 4; ++jp) {
            const int nu = 32 * ks + 8 * k1 + 2 * jp;
            const float r0 = (float)((nu * ka) & 63) * (1.f / 64), r1 = (float)(((nu + 1) * ka) & 63) * (1.f / 64);
            cb[0][ks][jp] = pk_f16(cf{cosr(r0), cosr(r1)});
            cb[1][ks][jp] = pk_f16(cf{-sinr(r0), -sinr(r1)});
        }
    {
        const int row = lane & 15, ko = row >> 2, jj = (row >> 1) & 1, outp = row & 1;
#pragma unroll
        for (int hi = 0; hi < 2; ++hi) {
            const int n1 = 4 * hi + k1;
            const float rv = (float)(n1 * ko) * (1.f / 7), ok = n1 < 7 ? 1.f : 0.f;
            const float c = cosr(rv) * ok, sn = sinr(rv) * ok;
            const float vre = outp ? -sn : c, vim = outp ? c : sn;
            const unsigned pre = jj ? pk_f16(cf{0.f, vre}) : pk_f16(cf{vre, 0.f}), pim = jj ? pk_f16(cf{0.f, vim}) : pk_f16(cf{vim, 0.f});
            ca[0][2 * hi] = pre; ca[0][2 * hi + 1] = 0u; ca[1][2 * hi] = pim; ca[1][2 * hi + 1] = 0u;
            ca[2][2 * hi] = 0u; ca[2][2 * hi + 1] = pre; ca[3][2 * hi] = 0u; ca[3][2 * hi + 1] = pim;
        }
    }
    Tw ts[8];
#pragma unroll
    for (int q = 1; q < 8; ++q) { const float rv = (float)(s0 * q) * (1.f / 2048); ts[q] = mktw(cf{cosr(rv), -sinr(rv)}); PIN(ts[q].w); PIN(ts[q].n); }
    unsigned wa[7];
    {
        int n1 = lane % 7;
#pragma unroll
        for (int e = 0; e < 7; ++e) {
            wa[e] = (16 * (w >> 1) + 2 * (w & 1) + 4 * n1 + 16 * (n1 >> 2)) * 160 + lane * 2;
            PIN(wa[e]);
            n1 = n1 == 6 ? 0 : n1 + 1;
        }
    }
    *(unsigned*)(smem + (44 + 16 * (tid >> 7) + ((tid >> 5) & 3)) * 160 + (tid & 31) * 4) = 0u;
    asm volatile("s_waitcnt vmcnt(0)" : "+v"(pfc), "+v"(v[0]), "+v"(v[1]), "+v"(v[2]), "+v"(v[3]), "+v"(v[4]), "+v"(v[5]), "+v"(v[6]),
                 "+v"(cb[0][0]), "+v"(cb[0][1]), "+v"(cb[1][0]), "+v"(cb[1][1]), "+v"(ca[0]), "+v"(ca[1]), "+v"(ca[2]), "+v"(ca[3]) :: "memory");
    STAMP(1);
    {
        cf mean, var;
        row_stats<7, false>(v, 1.f, mean, var);
        const cf rinv = rsq2(var + 1e-5f);
#pragma unroll
        for (int e = 0; e < 7; ++e) {
            const unsigned hz = pk_f16((v[e] - mean) * rinv);
            *(u16*)(smem + wa[e]) = (u16)hz; *(u16*)(smem + wa[e] + 160) = (u16)(hz >> 16);
        }
    }
    __syncthreads();
    STAMP(2);
    cf r[8];
    {
        u4 a[4][2];
#pragma unroll
        for (int rb = 0; rb < 4; ++rb) {
            const char* ap = smem + (16 * rb + (lane & 15)) * 160 + k1 * 16;
            a[rb][0] = *(const u4*)ap; a[rb][1] = *(const u4*)(ap + 64);
        }
        f4 c1[4][2];
#define MF_(d, x, y, c) "v_mfma_f32_16x16x32_f16 " d ", " x ", " y ", " c "\n\t"
        asm(MF_("%0", "%8", "%16", "0") MF_("%1", "%8", "%18", "0") MF_("%4", "%12", "%16", "0") MF_("%5", "%12", "%18", "0")
            MF_("%0", "%9", "%17", "%0") MF_("%1", "%9", "%19", "%1") MF_("%4", "%13", "%17", "%4") MF_("%5", "%13", "%19", "%5")
            MF_("%2", "%10", "%16", "0") MF_("%3", "%10", "%18", "0") MF_("%6", "%14", "%16", "0") MF_("%7", "%14", "%18", "0")
            MF_("%2", "%11", "%17", "%2") MF_("%3", "%11", "%19", "%3") MF_("%6", "%15", "%17", "%6") MF_("%7", "%15", "%19", "%7") "s_nop 7"
            : "=&v"(c1[0][0]), "=&v"(c1[0][1]), "=&v"(c1[1][0]), "=&v"(c1[1][1]), "=&v"(c1[2][0]), "=&v"(c1[2][1]), "=&v"(c1[3][0]), "=&v"(c1[3][1])
            : "v"(a[0][0]), "v"(a[0][1]), "v"(a[1][0]), "v"(a[1][1]), "v"(a[2][0]), "v"(a[2][1]), "v"(a[3][0]), "v"(a[3][1]),
              "v"(cb[0][0]), "v"(cb[0][1]), "v"(cb[1][0]), "v"(cb[1][1]));
        const u4 bre0 = cvt8(c1[0][0], c1[2][0]), bim0 = cvt8(c1[0][1], c1[2][1]);
        const u4 bre1 = cvt8(c1[1][0], c1[3][0]), bim1 = cvt8(c1[1][1], c1[3][1]);
        f4 o0, o1, o2, o3;
        asm("s_nop 1\n\t" MF_("%0", "%8", "%4", "0") MF_("%1", "%10", "%4", "0") MF_("%2", "%8", "%6", "0") MF_("%3", "%10", "%6", "0")
            MF_("%0", "%9", "%5", "%0") MF_("%1", "%11", "%5", "%1") MF_("%2", "%9", "%7", "%2") MF_("%3", "%11", "%7", "%3") "s_nop 7"
            : "=&v"(o0), "=&v"(o1), "=&v"(o2), "=&v"(o3)
            : "v"(bre0), "v"(bim0), "v"(bre1), "v"(bim1), "v"(ca[0]), "v"(ca[1]), "v"(ca[2]), "v"(ca[3]));
        r[0] = o0.xy; r[1] = o0.zw; r[2] = o1.xy; r[3] = o1.zw; r[4] = o2.xy; r[5] = o2.zw; r[6] = o3.xy; r[7] = o3.zw;
    }
    STAMP(3);
    dft8<false>(r);
    *(unsigned*)(ws + tw_off) = pk_f16(r[0]);
#pragma unroll
    for (int q = 1; q < 8; ++q) *(unsigned*)(ws + tw_off + q * (32 * 16384)) = pk_f16(cmul(r[q], ts[q]));
    STAMP(6);
    STAMP_FLUSH(0);
}

#define ACT_SC " sc1"
#define NA 2
#define NB 2
#define GPD 4
#define LDS_SCR 131072
#define LDS_TOTAL 163840
#define LD16(dst, voff, base) asm volatile("global_load_dwordx4 %0, %1, %2" : "=v"(dst) : "v"(voff), "s"(base) : "memory")
#define LD16O(dst, voff, base, imm) asm volatile("global_load_dwordx4 %0, %1, %2 offset:%3" : "=v"(dst) : "v"(voff), "s"(base), "i"(imm) : "memory")
#define DRAIN() asm volatile("s_waitcnt vmcnt(0)" ::: "memory")

template <bool G1, int T0, int N, int SL0 = 0>
__device__ __forceinline__ void stage_w(u4 srd, unsigned voff0, unsigned voff1, unsigned ldsbase, unsigned rowbytes) {
#pragma unroll
    for (int t = 0; t < N; ++t)
#pragma unroll
        for (int i = 0; i < 4; ++i) {
            const int row = G1 ? (4 * (T0 + t) + i) : (32 * (T0 + t) + 4 * i);
            unsigned tmp;
            asm volatile("s_add_i32 m0, %2, %4\n\ts_mul_i32 %0, %6, %5\n\tbuffer_load_dwordx4 %3, %1, %0 offen lds"
                         : "=&s"(tmp) : "s"(srd), "s"(ldsbase), "v"((i & 1) ? voff1 : voff0), "i"((SL0 + t) * 4096 + i * 1024), "i"(row), "s"(rowbytes) : "memory", "scc");
        }
}
template <bool DATA_A>
__device__ __forceinline__ void mf4(f4& r0, f4& i0, f4& r1, f4& i1, u4 d0, u4 d1, u4 mr, u4 mi) {
    if (DATA_A)
        asm("s_nop 1\n\tv_mfma_f32_16x16x32_f16 %0, %4, %6, 0\n\tv_mfma_f32_16x16x32_f16 %1, %4, %7, 0\n\t"
            "v_mfma_f32_16x16x32_f16 %2, %5, %6, 0\n\tv_mfma_f32_16x16x32_f16 %3, %5, %7, 0\n\ts_nop 7"
            : "=&v"(r0), "=&v"(i0), "=&v"(r1), "=&v"(i1) : "v"(d0), "v"(d1), "v"(mr), "v"(mi));
    else
        asm("s_nop 1\n\tv_mfma_f32_16x16x32_f16 %0, %6, %4, 0\n\tv_mfma_f32_16x16x32_f16 %1, %7, %4, 0\n\t"
            "v_mfma_f32_16x16x32_f16 %2, %6, %5, 0\n\tv_mfma_f32_16x16x32_f16 %3, %7, %5, 0\n\ts_nop 7"
            : "=&v"(r0), "=&v"(i0), "=&v"(r1), "=&v"(i1) : "v"(d0), "v"(d1), "v"(mr), "v"(mi));
}
__device__ __forceinline__ void mf8(f4& r, f4& im, const u4 (&a)[4], const u4 (&br)[4], const u4 (&bi)[4]) {
    asm("s_nop 1\n\tv_mfma_f32_16x16x32_f16 %0, %2, %6, 0\n\tv_mfma_f32_16x16x32_f16 %1, %2, %10, 0\n\t"
        "v_mfma_f32_16x16x32_f16 %0, %3, %7, %0\n\tv_mfma_f32_16x16x32_f16 %1, %3, %11, %1\n\t"
        "v_mfma_f32_16x16x32_f16 %0, %4, %8, %0\n\tv_mfma_f32_16x16x32_f16 %1, %4, %12, %1\n\t"
        "v_mfma_f32_16x16x32_f16 %0, %5, %9, %0\n\tv_mfma_f32_16x16x32_f16 %1, %5, %13, %1\n\ts_nop 7"
        : "=&v"(r), "=&v"(im)
        : "v"(a[0]), "v"(a[1]), "v"(a[2]), "v"(a[3]), "v"(br[0]), "v"(br[1]), "v"(br[2]), "v"(br[3]), "v"(bi[0]), "v"(bi[1]), "v"(bi[2]), "v"(bi[3]));
}
__device__ __forceinline__ f4 fma4(f4 a, f4 b, f4 c) { return __builtin_elementwise_fma(a, b, c); }
__device__ __forceinline__ u4 cmul_cvt(f4 re, f4 im, f4 tr, f4 ti) { return cvt8(fma4(im, -ti, re * tr), fma4(re, ti, im * tr)); }
template <int A0, int N>
__device__ __forceinline__ void move_w(const unsigned (&traddr)[4]) {
#pragma unroll
    for (int t = 0; t < N; ++t)
#pragma unroll
        for (int m = 0; m < 4; ++m)
#pragma unroll
            for (int hh = 0; hh < 2; ++hh)
                asm volatile("ds_read_b64_tr_b16 a[%c1:%c2], %0 offset:%c3" ::"v"(traddr[m]), "i"(16 * (A0 + t) + 4 * m + 2 * hh),
                             "i"(16 * (A0 + t) + 4 * m + 2 * hh + 1), "i"(t * 4096 + hh * 512) : "memory");
    asm volatile("s_waitcnt lgkmcnt(0)" ::: "memory");
}
__device__ __forceinline__ void publish(char* ws, int seam, int idx, int tid) {
    if (tid < 8) __hip_atomic_store((GAS unsigned*)(ws + WS_FLAGS + (seam * 8 + tid) * 4096 + idx * 16), 1u, RLX_AGENT);
}
__device__ __forceinline__ void poll(char* ws, int seam, int rep, int idx) {
    GAS unsigned* p = (GAS unsigned*)(ws + WS_FLAGS + (seam * 8 + rep) * 4096 + idx * 16);
    for (int spin = 0; spin < (1 << 16); ++spin) {
        unsigned v = __hip_atomic_load(p, RLX_AGENT);
        if (__all(v != 0u)) break;
    }
    __builtin_amdgcn_fence(__ATOMIC_ACQUIRE, "wavefront");
}

template <bool G1, int NK, int PD, typename F>
__device__ __forceinline__ void gemm_loop(f4 (&acc)[4][4], const char* abase, unsigned aoff0, unsigned aoff1, F&& after_issue) {
    u4 bfr[NK][4];
    auto issue1 = [&](int t, int n) {
        const char* ab = abase + (size_t)t * (28 * 1024);
        asm volatile("global_load_dwordx4 %0, %1, %2 offset:%3" ACT_SC : "=v"(bfr[t][n]) : "v"(n < 2 ? aoff0 : aoff1), "s"(ab), "i"((n & 1) * 1024) : "memory");
    };
#pragma unroll
    for (int t = 0; t < PD; ++t)
#pragma unroll
        for (int n = 0; n < 4; ++n) issue1(t, n);
    after_issue();
#pragma unroll
    for (int t = 0; t < NK; ++t) {
        const int last = (t + PD - 1 < NK - 1) ? t + PD - 1 : NK - 1;
        asm volatile("s_waitcnt vmcnt(%4)" : "+v"(bfr[t][0]), "+v"(bfr[t][1]), "+v"(bfr[t][2]), "+v"(bfr[t][3]) : "i"((last - t) * 4) : "memory");
#pragma unroll
        for (int n = 0; n < 4; ++n) {
#pragma unroll
            for (int m = 0; m < 4; ++m) {
                if (t == 0) {
                    if (G1) asm volatile("v_mfma_f32_16x16x32_bf16 %0, a[%c2:%c3], %1, 0" : "=&v"(acc[m][n]) : "v"(bfr[t][n]), "i"(16 * t + 4 * m), "i"(16 * t + 4 * m + 3));
                    else asm volatile("v_mfma_f32_16x16x32_bf16 %0, %1, a[%c2:%c3], 0" : "=&v"(acc[m][n]) : "v"(bfr[t][n]), "i"(16 * t + 4 * m), "i"(16 * t + 4 * m + 3));
                } else {
                    if (G1) asm volatile("v_mfma_f32_16x16x32_bf16 %0, a[%c2:%c3], %1, %0" : "+v"(acc[m][n]) : "v"(bfr[t][n]), "i"(16 * t + 4 * m), "i"(16 * t + 4 * m + 3));
                    else asm volatile("v_mfma_f32_16x16x32_bf16 %0, %1, a[%c2:%c3], %0" : "+v"(acc[m][n]) : "v"(bfr[t][n]), "i"(16 * t + 4 * m), "i"(16 * t + 4 * m + 3));
                }
                if (m == 1 && t + PD < NK) issue1(t + PD, n);
            }
        }
    }
    asm volatile("s_nop 7\n\ts_nop 1" : "+v"(acc[0][0]), "+v"(acc[0][1]), "+v"(acc[0][2]), "+v"(acc[0][3]), "+v"(acc[1][0]), "+v"(acc[1][1]), "+v"(acc[1][2]), "+v"(acc[1][3]),
                 "+v"(acc[2][0]), "+v"(acc[2][1]), "+v"(acc[2][2]), "+v"(acc[2][3]), "+v"(acc[3][0]), "+v"(acc[3][1]), "+v"(acc[3][2]), "+v"(acc[3][3]));
}
__device__ __forceinline__ void reduce4(f4 (&acc)[4][4], char* smem, int wv, int lane) {
#pragma unroll
    for (int c = 1; c < 4; ++c) {
        char* dst = (c < 3 ? smem + LDS_SCR + (wv * 2 + c - 1) * 4096 : smem + wv * 32768 + 7 * 4096) + lane * 16;
#pragma unroll
        for (int mi = 0; mi < 2; ++mi)
#pragma unroll
            for (int ni = 0; ni < 2; ++ni) *(f4*)(dst + (mi * 2 + ni) * 1024) = acc[2 * (c >> 1) + mi][2 * (c & 1) + ni];
    }
    __syncthreads();
#pragma unroll
    for (int c = 1; c < 4; ++c) {
        const int sw = wv ^ c;
        const char* src = (c < 3 ? smem + LDS_SCR + (sw * 2 + c - 1) * 4096 : smem + sw * 32768 + 7 * 4096) + lane * 16;
#pragma unroll
        for (int mi = 0; mi < 2; ++mi)
#pragma unroll
            for (int ni = 0; ni < 2; ++ni) acc[mi][ni] += *(const f4*)(src + (mi * 2 + ni) * 1024);
    }
}

extern "C" __global__ __launch_bounds__(256, 1) __attribute__((amdgpu_waves_per_eu(1, 1))) void kf_fused(char* __restrict__ ws, const char* __restrict__ x,
                                                   const char* __restrict__ w1, const char* __restrict__ b1,
                                                   const char* __restrict__ w2, const char* __restrict__ b2,
                                                   u16* __restrict__ out) {
    extern __shared__ __attribute__((aligned(16))) char smem[];
    asm volatile("" ::: "a0", "a255");
    const int tid = threadIdx.x, lane = tid & 63, b = blockIdx.x;
    const int wv = __builtin_amdgcn_readfirstlane(tid >> 6);
    const int xcd = b & 7, off = b >> 3;
    STAMP_DECL;
    STAMP(0);
    const auto rs = __builtin_amdgcn_make_buffer_rsrc(ws, 0, WS_END, 0x00020000);
    char* scr = smem + LDS_SCR;
    const int gq = lane >> 4, col = lane & 15;
    u4 tw4[4], cm[4], wmx;
    f4 twc, tws;
    {
        const char* tb = ws + WS_T + b * 16384;
        unsigned o = (unsigned)((64 * gq + col) * 64 + wv * 16);
        LD16O(tw4[0], o, tb, 0); LD16O(tw4[1], o, tb, 1024); LD16O(tw4[2], o, tb, 2048); LD16O(tw4[3], o, tb, 3072);
        unsigned o1 = lane * 16;
        LD16(wmx, o1, ws + WS_WMAX);
        __builtin_amdgcn_sched_barrier(0);
    }
    {
        float c16[4], s16[4];
#pragma unroll
        for (int e = 0; e < 4; ++e) {
            const int n = col * (4 * gq + e);
            const float r16 = (float)(n & 15) * (1.f / 16), r256 = (float)n * (1.f / 256);
            c16[e] = cosr(r16); s16[e] = sinr(r16);
            twc[e] = cosr(r256) * 0.0625f; tws[e] = sinr(r256) * 0.0625f;
        }
        const unsigned c01 = pk_f16(cf{c16[0], c16[1]}), c23 = pk_f16(cf{c16[2], c16[3]});
        const unsigned s01 = pk_f16(cf{s16[0], s16[1]}), s23 = pk_f16(cf{s16[2], s16[3]});
        const unsigned n01 = s01 ^ 0x80008000u, n23 = s23 ^ 0x80008000u;
        cm[0] = u4{c01, c23, s01, s23}; cm[1] = u4{n01, n23, c01, c23};
        cm[2] = u4{c01, c23, n01, n23}; cm[3] = u4{s01, s23, c01, c23};
    }
    const int s0 = s0_of_block(b);
    const int dh = xcd & 1, o3 = off / 3, r3 = off - 3 * o3;
    const bool has1 = off < 25 - dh, has2 = off < 28;
    const int db1 = off < 21 ? 4 * dh + r3 : 3, db2 = off < 24 ? 4 * dh + r3 : 3;
    const int nb1 = 7 * (xcd >> 1) + (off < 21 ? o3 : has1 ? 4 * dh + off - 21 : 0);
    const int nb2 = 8 * (xcd >> 1) + (off < 24 ? o3 : has2 ? 4 * dh + off - 24 : 0);
    const int lc = lane < 56 ? lane : 55;
    const unsigned xoff = (unsigned)((s0 + 512 * wv) * DN + lc * 8) * 2u;
    const int mb = 2 * (wv >> 1);
    const unsigned ldsbase = (unsigned)(uintptr_t)smem + wv * 32768;
    unsigned v1o[2], v2o[2], traddr[4];
    {
        const int j = lane >> 3, cs = lane & 7;
#pragma unroll
        for (int par = 0; par < 2; ++par) {
            const int c = cs ^ (2 * (((j >> 1) & 1) | (par << 1)));
            v1o[par] = (unsigned)(256 * j * FN + nb1 * 64 + 8 * c) * 2u;
            v2o[par] = (unsigned)((16 * (j >> 2) + (j & 3)) * SN + nb2 * 64 + 8 * c) * 2u;
        }
        const int qq = (lane >> 2) & 3, pp = lane & 3;
        const int phi = ((qq >> 1) & 1) | ((gq & 1) << 1);
#pragma unroll
        for (int m = 0; m < 4; ++m) traddr[m] = ldsbase + (8 * gq + qq) * 128 + (((m ^ mb) ^ phi) * 32) + pp * 8;
    }
    u4 w1w = make_srd(w1 + (size_t)wv * (64 * FN * 2), (unsigned)(SN * FN * 2 - wv * (64 * FN * 2)));
    unsigned row1 = FN * 2;
    if (!has1) {
        const int pi = off >= 25 ? off - 25 : 0;
        w1w = make_srd(w2 + (size_t)(256 * pi + 64 * wv) * (SN * 2) + (xcd >> 1) * 1024, off >= 25 ? (unsigned)(63 * SN * 2 + 1024) : 0u);
        v1o[0] = v1o[1] = (unsigned)(lane * 16);
        row1 = SN * 2;
    }
    const u4 w2w = make_srd(w2 + (size_t)wv * (448 * SN * 2), (unsigned)(SN * FN * 2 - wv * (448 * SN * 2)));
    STAMP(8);
    stage_w<true, 0, NA>(w1w, v1o[0], v1o[1], ldsbase, row1);
    STAMP(9);
    asm volatile("s_waitcnt vmcnt(%11)" : "+v"(tw4[0]), "+v"(tw4[1]), "+v"(tw4[2]), "+v"(tw4[3]), "+v"(cm[0]), "+v"(cm[1]), "+v"(cm[2]), "+v"(cm[3]),
                 "+v"(twc), "+v"(tws), "+v"(wmx) : "i"(NA * 4) : "memory");
    STAMP(1);
    unsigned wexp;
    {
        unsigned m01 = wmx.x > wmx.y ? wmx.x : wmx.y, m23 = wmx.z > wmx.w ? wmx.z : wmx.w;
        wexp = wave_max(m01 > m23 ? m01 : m23) >> 7;
        wexp = wexp > 250u ? 250u : wexp;
    }
    const float wsc = __uint_as_float((252u - wexp) << 23);
    {
        u4 d0, d1;
        d0 = u4{__builtin_amdgcn_perm(tw4[1].x, tw4[0].x, 0x05040100u), __builtin_amdgcn_perm(tw4[3].x, tw4[2].x, 0x05040100u),
                __builtin_amdgcn_perm(tw4[1].x, tw4[0].x, 0x07060302u), __builtin_amdgcn_perm(tw4[3].x, tw4[2].x, 0x07060302u)};
        d1 = u4{__builtin_amdgcn_perm(tw4[1].y, tw4[0].y, 0x05040100u), __builtin_amdgcn_perm(tw4[3].y, tw4[2].y, 0x05040100u),
                __builtin_amdgcn_perm(tw4[1].y, tw4[0].y, 0x07060302u), __builtin_amdgcn_perm(tw4[3].y, tw4[2].y, 0x07060302u)};
        f4 r0, i0, r1, i1;
        mf4<true>(r0, i0, r1, i1, d0, d1, cm[0], cm[1]);
        d0 = cmul_cvt(r0, i0, twc, -tws); d1 = cmul_cvt(r1, i1, twc, -tws);
        mf4<false>(r0, i0, r1, i1, d0, d1, cm[0], cm[1]);
        f4 wr0, wi0, wr1, wi1;
#pragma unroll
        for (int e = 0; e < 4; ++e) {
            wr0[e] = __uint_as_float(tw4[e].z << 16); wi0[e] = __uint_as_float(tw4[e].z & 0xffff0000u);
            wr1[e] = __uint_as_float(tw4[e].w << 16); wi1[e] = __uint_as_float(tw4[e].w & 0xffff0000u);
        }
        d0 = cmul_cvt(r0, i0, wr0 * wsc, wi0 * wsc); d1 = cmul_cvt(r1, i1, wr1 * wsc, wi1 * wsc);
        mf4<true>(r0, i0, r1, i1, d0, d1, cm[2], cm[3]);
        d0 = cmul_cvt(r0, i0, twc, tws); d1 = cmul_cvt(r1, i1, twc, tws);
        mf4<false>(r0, i0, r1, i1, d0, d1, cm[2], cm[3]);
        char* dst = scr + (64 * gq + col) * 32 + ((wv ^ ((col >> 2) & 3)) * 8);
#pragma unroll
        for (int e = 0; e < 4; ++e) *(u2*)(dst + e * 512) = u2{pk_f16(cf{r0[e], i0[e]}), pk_f16(cf{r1[e], i1[e]})};
        __syncthreads();
        STAMP(10);
        const u4 o0 = *(const u4*)(scr + tid * 16), o1 = *(const u4*)(scr + 4096 + tid * 16);
        __builtin_amdgcn_raw_buffer_store_b128(o0, rs, tid * 16 + WS_T2 + b * 8192, 0, 16);
        __builtin_amdgcn_raw_buffer_store_b128(o1, rs, tid * 16 + WS_T2 + 4096 + b * 8192, 0, 16);
    }
    STAMP(2);
    const int ka = 16 * wv + col;
    Tw ts[8];
#pragma unroll
    for (int qq = 1; qq < 8; ++qq) { const float rv = (float)(s0 * qq) * (1.f / 2048); ts[qq] = mktw(cf{cosr(rv) * 0.25f, sinr(rv) * 0.25f}); PIN(ts[qq].w); PIN(ts[qq].n); }
    DRAIN();
    __syncthreads();
    STAMP(11);
    publish(ws, 0, b, tid);
    u2 bv[2]; unsigned bs[2];
    u4 ra, rb;
    {
        LD16(ra, xoff, x); LD16(rb, xoff, x + 256 * DN * 2);
        unsigned o = (unsigned)(nb1 * 64 + 16 * mb + 4 * gq) * 2u, ob = (unsigned)(nb2 * 64 + 16 * mb + col) * 2u;
        asm volatile("global_load_dwordx2 %0, %1, %2" : "=v"(bv[0]) : "v"(o), "s"(b1) : "memory");
        asm volatile("global_load_dwordx2 %0, %1, %2 offset:32" : "=v"(bv[1]) : "v"(o), "s"(b1) : "memory");
        asm volatile("global_load_ushort %0, %1, %2" : "=v"(bs[0]) : "v"(ob), "s"(b2) : "memory");
        asm volatile("global_load_ushort %0, %1, %2 offset:32" : "=v"(bs[1]) : "v"(ob), "s"(b2) : "memory");
    }
    u4 cbr[4], cbi[4], ca[2];
    stage_w<true, NA, 4 - NA, NA>(w1w, v1o[0], v1o[1], ldsbase, row1);
    __builtin_amdgcn_sched_barrier(0);
#define S3C(ks) do { _Pragma("unroll") for (int e = 0; e < 4; ++e) { \
            const float rv = (float)((ka * (16 * (ks) + 4 * gq + e)) & 63) * (1.f / 64); \
            const float c = cosr(rv) * 0.125f, sn = sinr(rv) * 0.125f; \
            cbr[ks][e] = pk_f16(cf{c, -sn}); cbi[ks][e] = pk_f16(cf{sn, c}); } \
        PIN(cbr[ks]); PIN(cbi[ks]); stage_w<true, 4 + (ks), 1, 4 + (ks)>(w1w, v1o[0], v1o[1], ldsbase, row1); __builtin_amdgcn_sched_barrier(0); } while (0)
    S3C(0); S3C(1); S3C(2); S3C(3);
#pragma unroll
    for (int f = 0; f < 2; ++f) {
        const int n1 = 4 * f + (col >> 2);
        const float rv = (float)(n1 * gq) * (1.f / 7), am = n1 < 7 ? (gq ? 2.f : 1.f) : 0.f;
        const float c = cosr(rv) * am, sn = -sinr(rv) * am;
        const unsigned pc = (col & 1) ? pk_f16(cf{0.f, c}) : pk_f16(cf{c, 0.f}), ps = (col & 1) ? pk_f16(cf{0.f, sn}) : pk_f16(cf{sn, 0.f});
        ca[f] = (col & 2) ? u4{0u, pc, 0u, ps} : u4{pc, 0u, ps, 0u};
        PIN(ca[f]);
    }
    poll(ws, 0, xcd, tid);
    asm volatile("" : "+v"(ra), "+v"(rb), "+v"(bv[0]), "+v"(bv[1]), "+v"(bs[0]), "+v"(bs[1]) :: "memory");
    __syncthreads();
    STAMP(3);
#define STG(i) stage_w<true, 8 + NB + (i), 1, NB + (i)>(w1w, v1o[0], v1o[1], ldsbase, row1)
    {
        unsigned tq[8];
        {
            const unsigned vo = (unsigned)(((8 * gq + (ka >> 3)) * 256 + s0) * 32 + ((((ka >> 1) & 3) ^ ((s0 >> 2) & 3)) * 8) + (ka & 1) * 4);
#pragma unroll
            for (int qq = 0; qq < 8; ++qq)
                asm volatile("global_load_dword %0, %1, %2 sc1" : "=v"(tq[qq]) : "v"(vo), "s"(ws + WS_T2 + qq * (32 * 8192)) : "memory");
        }
        move_w<0, 8>(traddr);
        stage_w<true, 8, NB>(w1w, v1o[0], v1o[1], ldsbase, row1);
        cf xv[8];
#pragma unroll
        for (int i = 0; i < 4; ++i) {
            xv[2 * i] = cf{__uint_as_float(ra[i] << 16), __uint_as_float(rb[i] << 16)};
            xv[2 * i + 1] = cf{__uint_as_float(ra[i] & 0xffff0000u), __uint_as_float(rb[i] & 0xffff0000u)};
            PIN(xv[2 * i]); PIN(xv[2 * i + 1]);
        }
        unsigned ya0, ya1, hxa;
        {
            int d0 = (64 * gq + 385 * ka) % DN;
            int d1 = d0 + 256; d1 = d1 >= DN ? d1 - DN : d1;
            if (gq == 3) d1 = DN + ka;
            ya0 = d0 * 8 + (d0 >> 3) * 16; ya1 = d1 * 8 + (d1 >> 3) * 16;
            hxa = lane * 128 + ((lane & 7) << 4) + wv * 4;
            PIN(ya0); PIN(ya1); PIN(hxa);
        }
        asm volatile("s_waitcnt vmcnt(%8)" : "+v"(tq[0]), "+v"(tq[1]), "+v"(tq[2]), "+v"(tq[3]), "+v"(tq[4]), "+v"(tq[5]), "+v"(tq[6]), "+v"(tq[7]) : "i"(NB * 4) : "memory");
        cf rr[8];
        rr[0] = unpk_f16(tq[0]) * 0.25f;
#pragma unroll
        for (int qq = 1; qq < 8; ++qq) rr[qq] = cmul(unpk_f16(tq[qq]), ts[qq]);
        STG(0);
        dft8<true>(rr);
        STAMP(12);
        {
            char* dst = scr + gq * (4 * 288) + ka * 4;
#pragma unroll
            for (int j = 0; j < 8; ++j) *(unsigned*)(dst + (16 * (j >> 2) + (j & 3)) * 288) = pk_f16(rr[j]);
        }
        STG(1);
        __syncthreads();
        u4 bop[2];
        {
            u4 a0[4], a1[4];
#pragma unroll
            for (int ks = 0; ks < 4; ++ks) a0[ks] = *(const u4*)(scr + col * 288 + ks * 64 + gq * 16);
#pragma unroll
            for (int ks = 0; ks < 4; ++ks) a1[ks] = *(const u4*)(scr + (16 + col) * 288 + ks * 64 + gq * 16);
            STG(2);
            f4 re, im;
            mf8(re, im, a0, cbr, cbi);
            bop[0] = cvt8(re, im);
            mf8(re, im, a1, cbr, cbi);
            bop[1] = cvt8(re, im);
        }
        f4 y00, y01, y10, y11;
        mf4<false>(y00, y01, y10, y11, bop[0], bop[1], ca[0], ca[1]);
        char* yx = scr + 32 * 288;
        *(cf*)(yx + ya0) = y00.xy; *(cf*)(yx + ya0 + 5120) = y00.zw; *(cf*)(yx + ya0 + 10240) = y10.xy; *(cf*)(yx + ya0 + 15360) = y10.zw;
        *(cf*)(yx + ya1) = y01.xy; *(cf*)(yx + ya1 + 5120) = y01.zw; *(cf*)(yx + ya1 + 10240) = y11.xy; *(cf*)(yx + ya1 + 15360) = y11.zw;
        STG(3);
        __syncthreads();
        const float ysc = __uint_as_float((wexp + 2u) << 23) * (32.0f * 256.0f / ((float)SN * (float)DN));
        f4 yy[4];
#pragma unroll
        for (int i = 0; i < 4; ++i) yy[i] = *(const f4*)(yx + wv * 5120 + lc * 80 + i * 16);
        STG(4);
        unsigned hp[8];
        cf v[8];
#pragma unroll
        for (int i = 0; i < 4; ++i) {
            hp[2 * i] = pk_bf16(cfma(yy[i].xy, cf{ysc, ysc}, xv[2 * i]));
            hp[2 * i + 1] = pk_bf16(cfma(yy[i].zw, cf{ysc, ysc}, xv[2 * i + 1]));
        }
#pragma unroll
        for (int e = 0; e < 8; ++e) v[e] = unpk_bf16(hp[e]);
        cf mean, var;
        row_stats<8, true>(v, lane < 56 ? 1.f : 0.f, mean, var);
        const cf r1 = rsq2(var + 1e-5f);
        const cf rr2 = r1 * rsq2(cfma(var * r1, r1, cf{1e-5f, 1e-5f}));
        char* hx = scr;
#pragma unroll
        for (int e = 0; e < 8; ++e) *(unsigned*)(hx + (hxa ^ (e << 4))) = pk_bf16((v[e] - mean) * rr2);
        {
            u4 ha, hb;
#pragma unroll
            for (int i = 0; i < 4; ++i) {
                ha[i] = __builtin_amdgcn_perm(hp[2 * i + 1], hp[2 * i], 0x05040100u);
                hb[i] = __builtin_amdgcn_perm(hp[2 * i + 1], hp[2 * i], 0x07060302u);
            }
            STG(5);
            __builtin_amdgcn_raw_buffer_store_b128(ha, rs, xoff + WS_H, 0, 16);
            __builtin_amdgcn_raw_buffer_store_b128(hb, rs, xoff + (WS_H + 256 * DN * 2), 0, 16);
        }
        __syncthreads();
#pragma unroll
        for (int it = 0; it < 2; ++it) {
            if (it == 0 || wv < 3) {
                const int P = tid + it * 256;
                const int n = P >> 4, rho = P & 15;
                const int d = 32 * (n >> 1) + 8 * (rho >> 2) + 4 * (n & 1) + (rho & 3);
                u4 val = *(const u4*)(hx + (d >> 3) * 128 + (((d & 7) ^ ((d >> 3) & 7)) << 4));
                __builtin_amdgcn_raw_buffer_store_b128(val, rs, n * 1024 + rho * 16 + WS_HP + (s0 >> 2) * (28 * 1024) + (s0 & 3) * 256, 0, 16);
            }
        }
    }
    STAMP(4);
    asm volatile("s_waitcnt vmcnt(1)" ::: "memory");
    move_w<8, 8>(traddr);
    DRAIN();
    __syncthreads();
    STAMP(13);
    publish(ws, 1, s0, tid);
    stage_w<false, 0, 7>(w2w, v2o[0], v2o[1], ldsbase, (unsigned)(SN * 2));
    const unsigned aoff0 = (unsigned)lane * 16u + (wv & 1) * 2048u, aoff1 = (unsigned)lane * 16u + (1 - (wv & 1)) * 2048u;
    f4 acc[4][4];
    if (has1) {
        poll(ws, 1, xcd, tid);
        STAMP(14);
        gemm_loop<true, 16, GPD>(acc, ws + WS_HP + db1 * 4096 + (size_t)wv * (16 * 28 * 1024), aoff0, aoff1, [] {});
        STAMP(5);
        reduce4(acc, smem, wv, lane);
#pragma unroll
        for (int ni = 0; ni < 2; ++ni) {
            u4 o;
#pragma unroll
            for (int mi = 0; mi < 2; ++mi)
#pragma unroll
                for (int hh = 0; hh < 2; ++hh) {
                    cf a = hh ? acc[mi][ni].zw : acc[mi][ni].xy;
                    cf vv = rbf2(rbf2(a) + unpk_bf16(bv[mi][hh]));
                    cf zz = vv * cfma(vv * vv, cf{0.044715f, 0.044715f}, cf{1.f, 1.f}) * (-2.0f * 0.7978845608028654f * 1.4426950408889634f);
                    cf ee = cf{__builtin_amdgcn_exp2f(zz.x), __builtin_amdgcn_exp2f(zz.y)} + 1.0f;
                    cf gg = vv * cf{__builtin_amdgcn_rcpf(ee.x), __builtin_amdgcn_rcpf(ee.y)};
                    o[mi * 2 + hh] = pk_bf16(gg);
                }
            const int ks = nb1 * 2 + (wv >> 1);
            __builtin_amdgcn_raw_buffer_store_b128(o, rs, lane * 16 + ni * 1024 + WS_UP + (ks * 28 + db1 * 4 + 2 * (wv & 1)) * 1024, 0, 16);
        }
    } else {
        DRAIN();
    }
    if (!has2) return;
    move_w<0, 7>(traddr);
    DRAIN();
    __syncthreads();
    STAMP(15);
    if (has1) publish(ws, 2, db1 * 28 + nb1, tid);
    stage_w<false, 7, 7>(w2w, v2o[0], v2o[1], ldsbase, (unsigned)(SN * 2));
    poll(ws, 2, xcd, db2 * 28 + wv * 7 + (lane < 7 ? lane : 0));
    STAMP(6);
    u4 hv[2];
    const unsigned ho = (unsigned)((nb2 * 64 + 16 * mb + col) * DN + db2 * 64 + 32 * (wv & 1) + 8 * gq) * 2u;
    asm volatile("global_load_dwordx4 %0, %1, %2 sc1" : "=v"(hv[0]) : "v"(ho), "s"(ws + WS_H) : "memory");
    asm volatile("global_load_dwordx4 %0, %1, %2 sc1" : "=v"(hv[1]) : "v"(ho), "s"(ws + WS_H + 16 * DN * 2) : "memory");
    gemm_loop<false, 14, GPD>(acc, ws + WS_UP + db2 * 4096 + (size_t)wv * (14 * 28 * 1024), aoff0, aoff1, [&] { move_w<7, 7>(traddr); });
    asm volatile("" : "+v"(hv[0]), "+v"(hv[1]));
    reduce4(acc, smem, wv, lane);
#pragma unroll
    for (int mi = 0; mi < 2; ++mi) {
        const float bb = __uint_as_float(bs[mi] << 16);
        u4 o;
#pragma unroll
        for (int ni = 0; ni < 2; ++ni)
#pragma unroll
            for (int hh = 0; hh < 2; ++hh) {
                cf a = hh ? acc[mi][ni].zw : acc[mi][ni].xy;
                cf vv = rbf2(rbf2(a) + bb);
                o[ni * 2 + hh] = pk_bf16(unpk_bf16(hv[mi][ni * 2 + hh]) + vv);
            }
        *(u4*)((char*)out + ho + mi * (16 * DN * 2)) = o;
    }
    asm volatile(".Lkf_code_end:" ::: "memory");
    STAMP_FLUSH(2);
}

extern "C" void kernel_launch(void* const* d_in, const int* in_sizes, int n_in,
                              void* d_out, int out_size, void* d_ws, size_t ws_size,
                              hipStream_t stream) {
    const char* x = (const char*)d_in[0];
    const char* wr = (const char*)d_in[1];
    const char* wi = (const char*)d_in[2];
    const char* w1 = (const char*)d_in[4];
    const char* b1 = (const char*)d_in[5];
    const char* w2 = (const char*)d_in[6];
    const char* b2 = (const char*)d_in[7];
    char* ws = (char*)d_ws;
    (void)hipFuncSetAttribute((const void*)kf_fused, hipFuncAttributeMaxDynamicSharedMemorySize, LDS_TOTAL);
    k1_rowfft<<<512, 256, 0, stream>>>((const u16*)x, ws, wr, wi, w1, w2);
    kf_fused<<<256, 256, LDS_TOTAL, stream>>>(ws, x, w1, b1, w2, b2, (u16*)d_out);
}
```

```cpp
#include <hip/hip_runtime.h>
#include <hip/hip_bf16.h>
#include <stdint.h>

typedef unsigned short u16;
typedef float cf __attribute__((ext_vector_type(2)));
typedef float f4 __attribute__((ext_vector_type(4)));
typedef unsigned u4 __attribute__((ext_vector_type(4)));
typedef unsigned u2 __attribute__((ext_vector_type(2)));
typedef __bf16 bf2 __attribute__((ext_vector_type(2)));
typedef __bf16 bf8 __attribute__((ext_vector_type(8)));
typedef _Float16 h2 __attribute__((ext_vector_type(2)));
typedef _Float16 h8 __attribute__((ext_vector_type(8)));

#define SN 2048
#define DN 448
#define DFQ 225
#define FN 1792

#define STAMP_DECL
#define STAMP(i) do {} while (0)
#define STAMP_FLUSH(k) do {} while (0)
#define WSYNC() do { __builtin_amdgcn_fence(__ATOMIC_ACQ_REL, "wavefront"); __builtin_amdgcn_wave_barrier(); } while (0)
__device__ __forceinline__ cf cfma(cf a, cf b, cf c) { return __builtin_elementwise_fma(a, b, c); }
__device__ __forceinline__ cf addi(cf a, cf b) { return cfma(b.yx, cf{-1.f, 1.f}, a); }
__device__ __forceinline__ cf subi(cf a, cf b) { return cfma(b.yx, cf{1.f, -1.f}, a); }
struct Tw { cf w; cf n; };
__device__ __forceinline__ Tw mktw(cf w) { Tw t; t.w = w; t.n = cf{-w.y, w.y}; return t; }
__device__ __forceinline__ cf cmul(cf a, const Tw& t) { return cfma(a.yx, t.n, a * t.w.xx); }
__device__ __forceinline__ cf cmulc(cf a, const Tw& t) { return cfma(a.yx, t.n.yx, a * t.w.xx); }

template <bool INV> __device__ __forceinline__ cf rot_m(cf a, cf b) { return INV ? addi(a, b) : subi(a, b); }
template <bool INV> __device__ __forceinline__ cf rot_p(cf a, cf b) { return INV ? subi(a, b) : addi(a, b); }

template <bool INV> __device__ __forceinline__ void dft8(cf (&x)[8]) {
    const float R = 0.70710678118654752440f;
    cf a0 = x[0] + x[4], a4 = x[0] - x[4], a1 = x[1] + x[5], a5 = x[1] - x[5];
    cf a2 = x[2] + x[6], a6 = x[2] - x[6], a3 = x[3] + x[7], a7 = x[3] - x[7];
    cf b0 = a0 + a2, b2 = a0 - a2, b1 = a1 + a3, b3 = a1 - a3;
    cf c0 = rot_m<INV>(a4, a6), c2 = rot_p<INV>(a4, a6);
    cf e = rot_m<INV>(a5, a7), f = rot_p<INV>(a5, a7);
    cf g = rot_m<INV>(e, e), h = rot_m<INV>(f, f);
    x[0] = b0 + b1; x[4] = b0 - b1;
    x[2] = rot_m<INV>(b2, b3); x[6] = rot_p<INV>(b2, b3);
    x[1] = cfma(g, cf{R, R}, c0); x[5] = cfma(g, cf{-R, -R}, c0);
    x[3] = cfma(h.yx, INV ? cf{-R, R} : cf{R, -R}, c2);
    x[7] = cfma(h.yx, INV ? cf{R, -R} : cf{-R, R}, c2);
}
__device__ __forceinline__ unsigned pk_f16(cf v) { return __builtin_bit_cast(unsigned, __builtin_convertvector(v, h2)); }
__device__ __forceinline__ cf unpk_f16(unsigned p) { return __builtin_convertvector(__builtin_bit_cast(h2, p), cf); }
__device__ __forceinline__ unsigned pk_bf16(cf v) { return __builtin_bit_cast(unsigned, __builtin_convertvector(v, bf2)); }
__device__ __forceinline__ cf unpk_bf16(unsigned p) { return cf{__uint_as_float(p << 16), __uint_as_float(p & 0xffff0000u)}; }
__device__ __forceinline__ cf rbf2(cf v) { return unpk_bf16(pk_bf16(v)); }

template <int CTRL, int RM> __device__ __forceinline__ float dpp_add(float v) {
    int t = __builtin_amdgcn_update_dpp(0, __float_as_int(v), CTRL, RM, 0xf, true);
    return v + __int_as_float(t);
}
__device__ __forceinline__ float swap32_add(float a, float b) {
    auto r = __builtin_amdgcn_permlane32_swap(__float_as_uint(a), __float_as_uint(b), false, false);
    return __uint_as_float(r[0]) + __uint_as_float(r[1]);
}
__device__ __forceinline__ float swap16_add(float a, float b) {
    auto r = __builtin_amdgcn_permlane16_swap(__float_as_uint(a), __float_as_uint(b), false, false);
    return __uint_as_float(r[0]) + __uint_as_float(r[1]);
}
__device__ __forceinline__ float rdlane(float v, int l) { return __int_as_float(__builtin_amdgcn_readlane(__float_as_int(v), l)); }
__device__ __forceinline__ void wave_sum4(cf& s, cf& q) {
    float v = swap16_add(swap32_add(s.x, s.y), swap32_add(q.x, q.y));
    v = dpp_add<0x111, 0xf>(v); v = dpp_add<0x112, 0xf>(v); v = dpp_add<0x114, 0xf>(v); v = dpp_add<0x118, 0xf>(v);
    s = cf{rdlane(v, 15), rdlane(v, 47)};
    q = cf{rdlane(v, 31), rdlane(v, 63)};
}

template <int NE, bool MASK>
__device__ __forceinline__ void row_stats(const cf (&v)[NE], float act, cf& mean, cf& var) {
    const cf piv = cf{rdlane(v[0].x, 0), rdlane(v[0].y, 0)};
    cf d[NE];
#pragma unroll
    for (int e = 0; e < NE; ++e) d[e] = v[e] - piv;
    cf s = ((d[0] + d[1]) + (d[2] + d[3])) + ((d[4] + d[5]) + (NE == 8 ? d[6] + d[NE - 1] : d[6]));
    cf q0 = cfma(d[2], d[2], d[0] * d[0]), q1 = cfma(d[3], d[3], d[1] * d[1]);
    q0 = cfma(d[4], d[4], q0); q1 = cfma(d[5], d[5], q1);
    q0 = cfma(d[6], d[6], q0);
    if (NE == 8) q1 = cfma(d[NE - 1], d[NE - 1], q1);
    cf q = q0 + q1;
    if (MASK) { q = q * act; s = s * act; }
    wave_sum4(s, q);
    const cf md = s * (1.0f / DN);
    var = cfma(-md, md, q * (1.0f / DN));
    var = cf{__builtin_fmaxf(var.x, 0.f), __builtin_fmaxf(var.y, 0.f)};
    mean = piv + md;
}
__device__ __forceinline__ cf rsq2(cf v) { return cf{__builtin_amdgcn_rsqf(v.x), __builtin_amdgcn_rsqf(v.y)}; }
__device__ __forceinline__ float cosr(float rev) { return __builtin_amdgcn_cosf(rev); }
__device__ __forceinline__ float sinr(float rev) { return __builtin_amdgcn_sinf(rev); }
#define PIN(x) asm volatile("" : "+v"(x))
template <int CTRL, int RM> __device__ __forceinline__ unsigned dpp_max(unsigned v) {
    unsigned t = (unsigned)__builtin_amdgcn_update_dpp(0, (int)v, CTRL, RM, 0xf, true);
    return v > t ? v : t;
}
__device__ __forceinline__ unsigned wave_max(unsigned v) {
    v = dpp_max<0x111, 0xf>(v); v = dpp_max<0x112, 0xf>(v); v = dpp_max<0x114, 0xf>(v); v = dpp_max<0x118, 0xf>(v);
    v = dpp_max<0x142, 0xa>(v); v = dpp_max<0x143, 0xc>(v);
    return (unsigned)__builtin_amdgcn_readlane((int)v, 63);
}

#define WS_FLAGS 0
#define WS_NONCE (4 * 8 * 4096)
#define WS_T   (1 << 20)
#define WS_WP  (3 << 20)
#define WS_T2  (5 << 20)
#define WS_H   (7 << 20)
#define WS_HP  (9 << 20)
#define WS_UP  (11 << 20)
#define WS_END (13 << 20)

typedef unsigned long long u64;
#define GAS __attribute__((address_space(1)))
#define RLX_AGENT __ATOMIC_RELAXED, __HIP_MEMORY_SCOPE_AGENT
#define PF_LOAD(dst, base, off) asm volatile("global_load_dword %0, %1, %2" : "=v"(dst) : "v"(off), "s"(base) : "memory")
__device__ __forceinline__ int s0_of_block(int b) { return (b & ~31) | ((b & 7) << 2) | ((b >> 3) & 3); }
__device__ __forceinline__ u4 cvt8(f4 a, f4 b) { return u4{pk_f16(a.xy), pk_f16(a.zw), pk_f16(b.xy), pk_f16(b.zw)}; }
__device__ __forceinline__ u4 make_srd(const char* base, unsigned bytes) {
    return u4{(unsigned)(uintptr_t)base, (unsigned)((uintptr_t)base >> 32) & 0xffffu, bytes, 0x00020000u};
}
#define GLDS_B(srd, voff, soff, ldsaddr) asm volatile("s_mov_b32 m0, %3\n\ts_nop 0\n\tbuffer_load_dwordx4 %0, %1, %2 offen lds" :: "v"(voff), "s"(srd), "s"(soff), "s"(ldsaddr) : "memory")
#define CODE_ADDR(dst, sym) do { unsigned long long pc_; int dl_; \
    asm volatile("s_getpc_b64 %0\n.Lpc_%=:\n\ts_mov_b32 %1, " sym "-.Lpc_%=" : "=s"(pc_), "=s"(dl_)); dst = (const char*)(pc_ + (long long)dl_); } while (0)
#define ACT_SC " sc1"
#define GPD 4
#define LDS_SCR 131072
#define LDS_TOTAL 163840
#define LD16(dst, voff, base) asm volatile("global_load_dwordx4 %0, %1, %2" : "=v"(dst) : "v"(voff), "s"(base) : "memory")
#define LD16C(dst, voff, base) asm volatile("global_load_dwordx4 %0, %1, %2 sc1" : "=v"(dst) : "v"(voff), "s"(base) : "memory")
#define DRAIN() asm volatile("s_waitcnt vmcnt(0)" ::: "memory")
#define MF_(d, x, y, c) "v_mfma_f32_16x16x32_f16 " d ", " x ", " y ", " c "\n\t"

template <bool G1, int N, int SL>
__device__ __forceinline__ void stage1(u4 srd, unsigned voff0, unsigned voff1, unsigned ldsbase, unsigned rowbytes) {
    constexpr int t = N >> 2, i = N & 3;
    constexpr int row = G1 ? 4 * t + i : 32 * t + 4 * i;
    unsigned tmp;
    asm volatile("s_add_i32 m0, %2, %4\n\ts_mul_i32 %0, %6, %5\n\tbuffer_load_dwordx4 %3, %1, %0 offen lds"
                 : "=&s"(tmp) : "s"(srd), "s"(ldsbase), "v"((i & 1) ? voff1 : voff0), "i"(SL * 4096 + i * 1024), "i"(row), "s"(rowbytes) : "memory", "scc");
}
template <bool G1, int N0, int CNT, int SLOFF>
__device__ __forceinline__ void stage_n(u4 srd, unsigned voff0, unsigned voff1, unsigned ldsbase, unsigned rowbytes) {
    if constexpr (CNT > 0) {
        stage1<G1, N0, (N0 >> 2) + SLOFF>(srd, voff0, voff1, ldsbase, rowbytes);
        stage_n<G1, N0 + 1, CNT - 1, SLOFF>(srd, voff0, voff1, ldsbase, rowbytes);
    }
}
template <bool DATA_A>
__device__ __forceinline__ void mf4(f4& r0, f4& i0, f4& r1, f4& i1, u4 d0, u4 d1, u4 mr, u4 mi) {
    if (DATA_A)
        asm("s_nop 1\n\tv_mfma_f32_16x16x32_f16 %0, %4, %6, 0\n\tv_mfma_f32_16x16x32_f16 %1, %4, %7, 0\n\t"
            "v_mfma_f32_16x16x32_f16 %2, %5, %6, 0\n\tv_mfma_f32_16x16x32_f16 %3, %5, %7, 0\n\ts_nop 7"
            : "=&v"(r0), "=&v"(i0), "=&v"(r1), "=&v"(i1) : "v"(d0), "v"(d1), "v"(mr), "v"(mi));
    else
        asm("s_nop 1\n\tv_mfma_f32_16x16x32_f16 %0, %6, %4, 0\n\tv_mfma_f32_16x16x32_f16 %1, %7, %4, 0\n\t"
            "v_mfma_f32_16x16x32_f16 %2, %6, %5, 0\n\tv_mfma_f32_16x16x32_f16 %3, %7, %5, 0\n\ts_nop 7"
            : "=&v"(r0), "=&v"(i0), "=&v"(r1), "=&v"(i1) : "v"(d0), "v"(d1), "v"(mr), "v"(mi));
}
__device__ __forceinline__ void mf8(f4& r, f4& im, const u4 (&a)[4], const u4 (&br)[4], const u4 (&bi)[4]) {
    asm("s_nop 1\n\tv_mfma_f32_16x16x32_f16 %0, %2, %6, 0\n\tv_mfma_f32_16x16x32_f16 %1, %2, %10, 0\n\t"
        "v_mfma_f32_16x16x32_f16 %0, %3, %7, %0\n\tv_mfma_f32_16x16x32_f16 %1, %3, %11, %1\n\t"
        "v_mfma_f32_16x16x32_f16 %0, %4, %8, %0\n\tv_mfma_f32_16x16x32_f16 %1, %4, %12, %1\n\t"
        "v_mfma_f32_16x16x32_f16 %0, %5, %9, %0\n\tv_mfma_f32_16x16x32_f16 %1, %5, %13, %1\n\ts_nop 7"
        : "=&v"(r), "=&v"(im)
        : "v"(a[0]), "v"(a[1]), "v"(a[2]), "v"(a[3]), "v"(br[0]), "v"(br[1]), "v"(br[2]), "v"(br[3]), "v"(bi[0]), "v"(bi[1]), "v"(bi[2]), "v"(bi[3]));
}
__device__ __forceinline__ f4 fma4(f4 a, f4 b, f4 c) { return __builtin_elementwise_fma(a, b, c); }
__device__ __forceinline__ u4 cmul_cvt(f4 re, f4 im, f4 tr, f4 ti) { return cvt8(fma4(im, -ti, re * tr), fma4(re, ti, im * tr)); }
template <int A, int SL, int R>
__device__ __forceinline__ void move1(const unsigned (&traddr)[4]) {
    asm volatile("ds_read_b64_tr_b16 a[%c1:%c2], %0 offset:%c3" ::"v"(traddr[R >> 1]), "i"(16 * A + 2 * R), "i"(16 * A + 2 * R + 1),
                 "i"(SL * 4096 + (R & 1) * 512) : "memory");
}
template <int A0, int SL0, int CNT>
__device__ __forceinline__ void move_n(const unsigned (&traddr)[4]) {
    if constexpr (CNT > 0) {
        move_n<A0, SL0, CNT - 1>(traddr);
        move1<A0 + ((CNT - 1) >> 3), SL0 + ((CNT - 1) >> 3), (CNT - 1) & 7>(traddr);
    }
}
#define LGKM0() asm volatile("s_waitcnt lgkmcnt(0)" ::: "memory")
__device__ __forceinline__ void publish(char* ws, int seam, int idx, int tid, u64 word) {
    if (tid < 8) __hip_atomic_store((GAS u64*)(ws + WS_FLAGS + (seam * 8 + tid) * 4096 + idx * 16), word, RLX_AGENT);
}
__device__ __forceinline__ unsigned poll(char* ws, int seam, int rep, int idx, u64 tag) {
    GAS u64* p = (GAS u64*)(ws + WS_FLAGS + (seam * 8 + rep) * 4096 + idx * 16);
    u64 v = 0;
    for (int spin = 0; spin < (1 << 16); ++spin) {
        v = __hip_atomic_load(p, RLX_AGENT);
        if (__all(((v ^ tag) >> 16) == 0ull)) break;
    }
    __builtin_amdgcn_fence(__ATOMIC_ACQUIRE, "wavefront");
    return (unsigned)v & 0xffffu;
}

template <bool G1, int NK, int PD, int MT0, int MA0, int T, int I>
__device__ __forceinline__ void gemm_mfma(f4 (&acc)[4][4], u4 (&bfr)[NK][4], const char* abase, unsigned aoff0, unsigned aoff1, const unsigned (&traddr)[4]) {
    constexpr int n = I >> 2, m = I & 3;
    if constexpr (I == 0) {
        constexpr int last = (T + PD - 1 < NK - 1) ? T + PD - 1 : NK - 1;
        asm volatile("s_waitcnt vmcnt(%4)" : "+v"(bfr[T][0]), "+v"(bfr[T][1]), "+v"(bfr[T][2]), "+v"(bfr[T][3]) : "i"((last - T) * 4) : "memory");
        if constexpr (T == MT0 + 7) LGKM0();
    }
    if constexpr (T == 0) {
        if (G1) asm volatile("v_mfma_f32_16x16x32_bf16 %0, a[%c2:%c3], %1, 0" : "=&v"(acc[m][n]) : "v"(bfr[T][n]), "i"(16 * T + 4 * m), "i"(16 * T + 4 * m + 3));
        else asm volatile("v_mfma_f32_16x16x32_bf16 %0, %1, a[%c2:%c3], 0" : "=&v"(acc[m][n]) : "v"(bfr[T][n]), "i"(16 * T + 4 * m), "i"(16 * T + 4 * m + 3));
    } else {
        if (G1) asm volatile("v_mfma_f32_16x16x32_bf16 %0, a[%c2:%c3], %1, %0" : "+v"(acc[m][n]) : "v"(bfr[T][n]), "i"(16 * T + 4 * m), "i"(16 * T + 4 * m + 3));
        else asm volatile("v_mfma_f32_16x16x32_bf16 %0, %1, a[%c2:%c3], %0" : "+v"(acc[m][n]) : "v"(bfr[T][n]), "i"(16 * T + 4 * m), "i"(16 * T + 4 * m + 3));
    }
    if constexpr (m == 1 && T + PD < NK) {
        const char* ab = abase + (size_t)(T + PD) * (28 * 1024);
        asm volatile("global_load_dwordx4 %0, %1, %2 offset:%3 sc1" : "=v"(bfr[T + PD][n]) : "v"(n < 2 ? aoff0 : aoff1), "s"(ab), "i"((n & 1) * 1024) : "memory");
    }
    if constexpr ((m == 2 || m == 3) && T >= MT0 && T < MT0 + 7) move1<MA0 + T - MT0, T - MT0, 2 * n + (m - 2)>(traddr);
    if constexpr (I < 15) gemm_mfma<G1, NK, PD, MT0, MA0, T, I + 1>(acc, bfr, abase, aoff0, aoff1, traddr);
    else if constexpr (T + 1 < NK) gemm_mfma<G1, NK, PD, MT0, MA0, T + 1, 0>(acc, bfr, abase, aoff0, aoff1, traddr);
}
template <bool G1, int NK, int PD, int MT0, int MA0>
__device__ __forceinline__ void gemm_loop(f4 (&acc)[4][4], const char* abase, unsigned aoff0, unsigned aoff1, const unsigned (&traddr)[4]) {
    u4 bfr[NK][4];
#pragma unroll
    for (int t = 0; t < PD; ++t)
#pragma unroll
        for (int n = 0; n < 4; ++n) {
            const char* ab = abase + (size_t)t * (28 * 1024);
            asm volatile("global_load_dwordx4 %0, %1, %2 offset:%3 sc1" : "=v"(bfr[t][n]) : "v"(n < 2 ? aoff0 : aoff1), "s"(ab), "i"((n & 1) * 1024) : "memory");
        }
    gemm_mfma<G1, NK, PD, MT0, MA0, 0, 0>(acc, bfr, abase, aoff0, aoff1, traddr);
    asm volatile("s_nop 7\n\ts_nop 1" : "+v"(acc[0][0]), "+v"(acc[0][1]), "+v"(acc[0][2]), "+v"(acc[0][3]), "+v"(acc[1][0]), "+v"(acc[1][1]), "+v"(acc[1][2]), "+v"(acc[1][3]),
                 "+v"(acc[2][0]), "+v"(acc[2][1]), "+v"(acc[2][2]), "+v"(acc[2][3]), "+v"(acc[3][0]), "+v"(acc[3][1]), "+v"(acc[3][2]), "+v"(acc[3][3]));
}

#define SB() __builtin_amdgcn_sched_barrier(0)
#define A1(N, SLOFF) do { SB(); stage1<true, N, ((N) >> 2) + (SLOFF)>(w1w, v1o[0], v1o[1], ldsbase, row1); SB(); } while (0)
#define A2(N, SLOFF) do { SB(); stage1<false, N, ((N) >> 2) + (SLOFF)>(w2w, v2o[0], v2o[1], ldsbase, (unsigned)(SN * 2)); SB(); } while (0)
#define N1(N0, CNT, SLOFF) stage_n<true, N0, CNT, SLOFF>(w1w, v1o[0], v1o[1], ldsbase, row1)
#define N2(N0, CNT, SLOFF) stage_n<false, N0, CNT, SLOFF>(w2w, v2o[0], v2o[1], ldsbase, (unsigned)(SN * 2))
#define PFW(srd, i) do { SB(); GLDS_B(srd, vpf, (unsigned)((i) * 1024), dump); SB(); } while (0)

extern "C" __global__ __launch_bounds__(256, 1) __attribute__((amdgpu_waves_per_eu(1, 1))) void kf_fused(
        char* __restrict__ ws, const char* __restrict__ x, const char* __restrict__ wr, const char* __restrict__ wi,
        const char* __restrict__ w1, const char* __restrict__ b1, const char* __restrict__ w2, const char* __restrict__ b2,
        u16* __restrict__ out) {
    extern __shared__ __attribute__((aligned(16))) char smem[];
    asm volatile("" ::: "a0", "a255");
    const int tid = threadIdx.x, lane = tid & 63, b = blockIdx.x;
    const int wv = __builtin_amdgcn_readfirstlane(tid >> 6);
    const int xcd = b & 7, off = b >> 3, s0 = s0_of_block(b);
    STAMP_DECL;
    STAMP(0);
    char* scr = smem + LDS_SCR;
    const unsigned ldsbase = (unsigned)(uintptr_t)smem + wv * 32768;
    const unsigned dump = (unsigned)(uintptr_t)smem + LDS_SCR + 28672 + wv * 1024;
    const int gq = lane >> 4, col = lane & 15, ka = 16 * wv + col;
    unsigned pfc;
    {
        const char* code; CODE_ADDR(code, "kf_fused");
        unsigned clen;
        asm volatile("s_mov_b32 %0, .Lkf_code_end-kf_fused" : "=s"(clen));
        const unsigned cl = (unsigned)((off & 7) * 32 + wv * 8 + (lane & 7)) * 128u;
        const unsigned oc = cl < clen ? cl : 0u;
        PF_LOAD(pfc, code, oc);
    }
    cf v7[7];
    u2 nonce;
    {
        const char* xa = x + (size_t)(s0 + 512 * wv) * (DN * 2);
        const unsigned o = lane * 2;
#pragma unroll
        for (int e = 0; e < 7; ++e) {
            float a = 0.f, c = 0.f;
            asm volatile("global_load_short_d16_hi %0, %1, %2 offset:%3" : "+v"(a) : "v"(o), "s"(xa), "i"(128 * e) : "memory");
            asm volatile("global_load_short_d16_hi %0, %1, %2 offset:%3" : "+v"(c) : "v"(o), "s"(xa + 256 * DN * 2), "i"(128 * e) : "memory");
            v7[e] = cf{a, c};
        }
        const unsigned z = 0;
        asm volatile("global_load_dwordx2 %0, %1, %2 sc1" : "=v"(nonce) : "v"(z), "s"(ws + WS_NONCE) : "memory");
        SB();
    }
    const auto rs = __builtin_amdgcn_make_buffer_rsrc(ws, 0, WS_END, 0x00020000);
    const int hs0 = 2 * ((s0 >> 3) & 1) + ((s0 >> 6) & 1);
    const unsigned vpf = lane * 16 + b * 28672 + wv * 7168;
    const unsigned sgk = (ka & 1) ? 0x80008000u : 0u;
    Tw ts[8];
    u64 tag;
    {
        const u4 srdw = make_srd((wv & 1) ? wi : wr, SN * DFQ * 2);
        const u4 s1 = make_srd(w1, SN * FN * 2);
        const unsigned wso = (unsigned)(((wv >> 1) ? 255 - s0 : s0) * 3600), vo16 = lane * 16;
#define WROW(i) do { SB(); GLDS_B(srdw, vo16, wso + (i) * 1024, ldsbase + 28672 + (i) * 1024); SB(); } while (0)
        u4 cb[2][2], ca[4];
#define S1C(jp) do { const int nu = 8 * gq + 2 * (jp); \
            const float r0 = (float)((nu * ka) & 63) * (1.f / 64), r1 = (float)(((nu + 1) * ka) & 63) * (1.f / 64); \
            cb[0][0][jp] = pk_f16(cf{cosr(r0), cosr(r1)}); cb[0][1][jp] = cb[0][0][jp] ^ sgk; \
            cb[1][0][jp] = pk_f16(cf{-sinr(r0), -sinr(r1)}); cb[1][1][jp] = cb[1][0][jp] ^ sgk; } while (0)
        WROW(0); S1C(0); WROW(1); S1C(1); WROW(2); S1C(2); WROW(3); S1C(3);
        PFW(s1, 0);
        {
            const int ko = col >> 2, jj = (col >> 1) & 1, outp = col & 1;
#pragma unroll
            for (int hi = 0; hi < 2; ++hi) {
                const int n1 = 4 * hi + gq;
                const float rv = (float)(n1 * ko) * (1.f / 7), ok = n1 < 7 ? 1.f : 0.f;
                const float c = cosr(rv) * ok, sn = sinr(rv) * ok;
                const unsigned pp = pk_f16(cf{outp ? -sn : c, outp ? c : sn});
                const unsigned pre = jj ? pp << 16 : pp & 0xffffu, pim = jj ? pp & 0xffff0000u : pp >> 16;
                ca[0][2 * hi] = pre; ca[0][2 * hi + 1] = 0u; ca[1][2 * hi] = pim; ca[1][2 * hi + 1] = 0u;
                ca[2][2 * hi] = 0u; ca[2][2 * hi + 1] = pre; ca[3][2 * hi] = 0u; ca[3][2 * hi + 1] = pim;
                if (hi == 0) PFW(s1, 1);
            }
        }
        PFW(s1, 2);
        {
            const float rv = (float)(s0 * (lane & 7)) * (1.f / 2048);
            const float c = cosr(rv), sn = sinr(rv);
#pragma unroll
            for (int q = 1; q < 8; ++q) ts[q] = mktw(cf{rdlane(c, q), rdlane(sn, q)});
        }
        PFW(s1, 3);
        unsigned wa[7];
        {
            const unsigned base = (32 * (wv >> 1) + 2 * (wv & 1)) * 160 + lane * 2, r7 = (unsigned)(lane % 7) * 640u;
#pragma unroll
            for (int e = 0; e < 7; ++e) {
                const unsigned u = r7 + e * 640u, u2_ = u - 4480u;
                wa[e] = base + (u < u2_ ? u : u2_);
                PIN(wa[e]);
            }
        }
        PFW(s1, 4);
        unsigned ga[2][4], gs[4];
        {
            const int G = lane >> 1;
            const int kb = 64 * (G >> 3) + 56 * (G & 7);
            const unsigned mo0 = (wv == 0 && s0 == 0) ? 28672u : 28672u + 65536u + (8 - wv) * 450u, mo1 = 28672u + 65536u + (4 - wv) * 450u;
            const unsigned do0 = 28672u + wv * 450u, do1 = do0 + 1800u;
#pragma unroll
            for (int j = 0; j < 4; ++j) {
                int kd = kb + 14 * ((2 * (lane & 1) + (j >> 1)) ^ hs0) + 7 * (j & 1); kd = kd >= DN ? kd - DN : kd;
                const bool mir = kd > 224;
                const unsigned cb2 = (unsigned)(mir ? DN - kd : kd) * 2u;
                gs[j] = mir ? 0x80000000u : 0u;
                ga[0][j] = cb2 + (mir ? mo0 : do0); ga[1][j] = cb2 + (mir ? mo1 : do1);
                PIN(ga[0][j]); PIN(ga[1][j]);
                if (j == 1) PFW(s1, 5);
            }
        }
        unsigned tia = 16384 + (8 * gq + (ka >> 3)) * 32 + ((((ka >> 1) & 3) ^ hs0) * 8) + (ka & 1) * 4;
        PIN(tia);
        PFW(s1, 6);
        {
            const u4 s2 = make_srd(w2, SN * FN * 2);
            const unsigned vp2 = lane * 16 + b * 3584 + wv * (448 * SN * 2);
#pragma unroll
            for (int i = 0; i < 4; ++i) GLDS_B(s2, vp2, (unsigned)(i * 1024), dump);
        }
        *(unsigned*)(scr + (28 + 32 * (tid >> 7) + ((tid >> 5) & 3)) * 160 + (tid & 31) * 4) = 0u;
        SB();
        asm volatile("s_waitcnt vmcnt(15)" : "+v"(pfc), "+v"(nonce), "+v"(v7[0]), "+v"(v7[1]), "+v"(v7[2]), "+v"(v7[3]), "+v"(v7[4]), "+v"(v7[5]), "+v"(v7[6]) :: "memory");
        STAMP(1);
        {
            cf mean, var;
            row_stats<7, false>(v7, 1.f, mean, var);
            const cf rinv = rsq2(var + 1e-5f), nm = -mean * rinv;
            SB();
            unsigned hz[7];
#pragma unroll
            for (int e = 0; e < 7; ++e) v7[e] = (v7[e] - mean) * rinv;
            SB();
#pragma unroll
            for (int e = 0; e < 7; ++e) hz[e] = pk_f16(v7[e]);
            SB();
#pragma unroll
            for (int e = 0; e < 7; ++e) { *(u16*)(scr + wa[e]) = (u16)hz[e]; *(u16*)(scr + wa[e] + 160) = (u16)(hz[e] >> 16); }
            (void)nm;
        }
        {
            const u64 n = ((u64)(unsigned)__builtin_amdgcn_readfirstlane((int)nonce.y) << 32) | (unsigned)__builtin_amdgcn_readfirstlane((int)nonce.x);
            tag = ((n >> 16) * 0x5DEECE66Dull + 0xB16B00B5F00Dull) << 16;
        }
        asm volatile("s_waitcnt vmcnt(11)" ::: "memory");
        __syncthreads();
        STAMP(2);
        u4 a[4][2];
#pragma unroll
        for (int rb = 0; rb < 4; ++rb) {
            const char* ap = scr + (16 * rb + col) * 160 + gq * 16;
            a[rb][0] = *(const u4*)ap; a[rb][1] = *(const u4*)(ap + 64);
        }
        SB();
        unsigned gr[2][4], gi[2][4];
        f4 c1[4][2];
#define MZ(d, x, y) do { asm volatile("v_mfma_f32_16x16x32_f16 %0, %1, %2, 0" : "=&v"(d) : "v"(x), "v"(y)); SB(); } while (0)
#define MA(d, x, y) do { asm volatile("v_mfma_f32_16x16x32_f16 %0, %1, %2, %0" : "+v"(d) : "v"(x), "v"(y)); SB(); } while (0)
#define GRD(i, j) do { gr[i][j] = *(const u16*)(smem + ga[i][j]); gi[i][j] = *(const u16*)(smem + ga[i][j] + 32768); SB(); } while (0)
        MZ(c1[0][0], a[0][0], cb[0][0]); GRD(0, 0); MZ(c1[0][1], a[0][0], cb[1][0]); GRD(0, 1);
        MZ(c1[1][0], a[1][0], cb[0][0]); GRD(0, 2); MZ(c1[1][1], a[1][0], cb[1][0]); GRD(0, 3);
        MA(c1[0][0], a[0][1], cb[0][1]); GRD(1, 0); MA(c1[0][1], a[0][1], cb[1][1]); GRD(1, 1);
        MA(c1[1][0], a[1][1], cb[0][1]); GRD(1, 2); MA(c1[1][1], a[1][1], cb[1][1]); GRD(1, 3);
        typedef unsigned short us2 __attribute__((ext_vector_type(2)));
        u4 wo[2];
        unsigned wp[2][4];
#define GCB(i, j) do { wp[i][j] = gr[i][j] | (gi[i][j] << 16); wo[i][j] = wp[i][j] ^ gs[j]; SB(); } while (0)
        MZ(c1[2][0], a[2][0], cb[0][0]); MZ(c1[2][1], a[2][0], cb[1][0]);
        MZ(c1[3][0], a[3][0], cb[0][0]); GCB(0, 0); MZ(c1[3][1], a[3][0], cb[1][0]); GCB(0, 1);
        MA(c1[2][0], a[2][1], cb[0][1]); GCB(0, 2); MA(c1[2][1], a[2][1], cb[1][1]); GCB(0, 3);
        MA(c1[3][0], a[3][1], cb[0][1]);
        __builtin_amdgcn_raw_buffer_store_b128(wo[0], rs, tid * 16 + WS_WP + s0 * 8192, 0, 16);
        SB();
        MA(c1[3][1], a[3][1], cb[1][1]);
        const u4 bre0 = cvt8(c1[0][0], c1[1][0]), bim0 = cvt8(c1[0][1], c1[1][1]);
        SB();
        asm volatile("s_nop 0");
        const u4 bre1 = cvt8(c1[2][0], c1[3][0]), bim1 = cvt8(c1[2][1], c1[3][1]);
        SB();
        f4 o0, o1, o2, o3;
        MZ(o0, ca[0], bre0); GCB(1, 0); MZ(o1, ca[2], bre0); GCB(1, 1);
        MZ(o2, ca[0], bre1); GCB(1, 2); MZ(o3, ca[2], bre1); GCB(1, 3);
        MA(o0, ca[1], bim0);
        __builtin_amdgcn_raw_buffer_store_b128(wo[1], rs, tid * 16 + 4096 + WS_WP + s0 * 8192, 0, 16);
        SB();
        MA(o1, ca[3], bim0); MA(o2, ca[1], bim1); MA(o3, ca[3], bim1);
        asm volatile("s_nop 7" : "+v"(o0), "+v"(o1), "+v"(o2), "+v"(o3));
        cf r[8] = {o0.xy, o0.zw, o1.xy, o1.zw, o2.xy, o2.zw, o3.xy, o3.zw};
        dft8<false>(r);
        SB();
        {
            cf tm[8];
#pragma unroll
            for (int q = 1; q < 8; ++q) tm[q] = r[q] * ts[q].w.xx;
            SB();
#pragma unroll
            for (int q = 1; q < 8; ++q) r[q] = cfma(r[q].yx, ts[q].n.yx, tm[q]);
        }
        SB();
#pragma unroll
        for (int q = 0; q < 8; ++q) *(unsigned*)(scr + tia + q * 1024) = pk_f16(r[q]);
        __syncthreads();
        const u4 t0 = *(const u4*)(scr + 16384 + tid * 16), t1 = *(const u4*)(scr + 20480 + tid * 16);
        __builtin_amdgcn_raw_buffer_store_b128(t0, rs, tid * 16 + WS_T + s0 * 8192, 0, 16);
        __builtin_amdgcn_raw_buffer_store_b128(t1, rs, tid * 16 + 4096 + WS_T + s0 * 8192, 0, 16);
        SB();
        us2 mx = {0, 0};
#pragma unroll
        for (int k = 0; k < 8; ++k) mx = __builtin_elementwise_max(mx, __builtin_bit_cast(us2, wp[k >> 2][k & 3] & 0x7fff7fffu));
        const unsigned wm = wave_max(mx.x > mx.y ? mx.x : mx.y);
        if (lane == 0) *(unsigned*)(scr + 26624 + wv * 4) = wm;
    }
    STAMP(3);
    const int dh = xcd & 1, o3 = off / 3, r3 = off - 3 * o3;
    const bool has1 = off < 25 - dh, has2 = off < 28;
    const int db1 = off < 21 ? 4 * dh + r3 : 3, db2 = off < 24 ? 4 * dh + r3 : 3;
    const int nb1 = 7 * (xcd >> 1) + (off < 21 ? o3 : has1 ? 4 * dh + off - 21 : 0);
    const int nb2 = 8 * (xcd >> 1) + (off < 24 ? o3 : has2 ? 4 * dh + off - 24 : 0);
    const int lc = lane < 56 ? lane : 55;
    const unsigned xoff = (unsigned)((s0 + 512 * wv) * DN + lc * 8) * 2u;
    const int mb = 2 * (wv >> 1);
    unsigned v1o[2], v2o[2], traddr[4];
    {
        const int j = lane >> 3, cs = lane & 7;
#pragma unroll
        for (int par = 0; par < 2; ++par) {
            const int c = cs ^ (2 * (((j >> 1) & 1) | (par << 1)));
            v1o[par] = (unsigned)(256 * j * FN + nb1 * 64 + 8 * c) * 2u;
            v2o[par] = (unsigned)((16 * (j >> 2) + (j & 3)) * SN + nb2 * 64 + 8 * c) * 2u;
            PIN(v1o[par]); PIN(v2o[par]);
        }
        const int qq = (lane >> 2) & 3, pp = lane & 3;
        const int phi = ((qq >> 1) & 1) | ((gq & 1) << 1);
#pragma unroll
        for (int m = 0; m < 4; ++m) { traddr[m] = ldsbase + (8 * gq + qq) * 128 + (((m ^ mb) ^ phi) * 32) + pp * 8; PIN(traddr[m]); }
    }
    u4 w1w = make_srd(w1 + (size_t)wv * (64 * FN * 2), (unsigned)(SN * FN * 2 - wv * (64 * FN * 2)));
    unsigned row1 = FN * 2;
    if (!has1) {
        w1w = make_srd(w1 + (size_t)(64 * wv + 32 + 256 * (31 - off)) * (FN * 2) + (xcd >> 1) * 896, 31 * FN * 2 + 896);
        v1o[0] = v1o[1] = lane * 16;
    }
    const u4 w2w = make_srd(w2 + (size_t)wv * (448 * SN * 2), has2 ? (unsigned)(SN * FN * 2 - wv * (448 * SN * 2)) : 0u);
    u4 cm[4];
    f4 twc, tws;
    {
        float c16[4], s16[4];
#pragma unroll
        for (int e = 0; e < 4; ++e) {
            const int n = col * (4 * gq + e);
            const float r16 = (float)(n & 15) * (1.f / 16), r256 = (float)n * (1.f / 256);
            c16[e] = cosr(r16); s16[e] = sinr(r16);
            twc[e] = cosr(r256) * 0.0625f; tws[e] = sinr(r256) * 0.0625f;
        }
        const unsigned c01 = pk_f16(cf{c16[0], c16[1]}), c23 = pk_f16(cf{c16[2], c16[3]});
        const unsigned s01 = pk_f16(cf{s16[0], s16[1]}), s23 = pk_f16(cf{s16[2], s16[3]});
        const unsigned n01 = s01 ^ 0x80008000u, n23 = s23 ^ 0x80008000u;
        cm[0] = u4{c01, c23, s01, s23}; cm[1] = u4{n01, n23, c01, c23};
        cm[2] = u4{c01, c23, n01, n23}; cm[3] = u4{s01, s23, c01, c23};
        PIN(cm[0]); PIN(cm[1]); PIN(cm[2]); PIN(cm[3]); PIN(twc); PIN(tws);
    }
    DRAIN();
    __syncthreads();
    STAMP(4);
    {
        const u4 m4 = *(const u4*)(scr + 26624);
        const unsigned m01 = m4.x > m4.y ? m4.x : m4.y, m23 = m4.z > m4.w ? m4.z : m4.w;
        publish(ws, 0, s0, tid, tag | (m01 > m23 ? m01 : m23));
    }
    u4 cbr[4], cbi[4], ca3[2];
#define S3C(ks, e) do { const float rv = (float)((ka * (16 * (ks) + 4 * gq + (e))) & 63) * (1.f / 64); \
            const float c = cosr(rv) * 0.125f, sn = sinr(rv) * 0.125f; \
            cbr[ks][e] = pk_f16(cf{c, -sn}); cbi[ks][e] = pk_f16(cf{sn, c}); cbr[ks + 2][e] = cbr[ks][e] ^ sgk; cbi[ks + 2][e] = cbi[ks][e] ^ sgk; } while (0)
    A1(0, 0); S3C(0, 0); A1(1, 0); S3C(0, 1); A1(2, 0); S3C(0, 2); A1(3, 0); S3C(0, 3);
    A1(4, 0); S3C(1, 0); A1(5, 0); S3C(1, 1); A1(6, 0); S3C(1, 2); A1(7, 0); S3C(1, 3);
    A1(8, 0);
#pragma unroll
    for (int f = 0; f < 2; ++f) {
        const int n1 = 4 * f + (col >> 2);
        const float rv = (float)(n1 * gq) * (1.f / 7), am = n1 < 7 ? (gq ? 2.f : 1.f) : 0.f;
        const unsigned pp = pk_f16(cf{cosr(rv) * am, -sinr(rv) * am});
        const unsigned pc = (col & 1) ? pp << 16 : pp & 0xffffu, ps = (col & 1) ? pp & 0xffff0000u : pp >> 16;
        ca3[f] = (col & 2) ? u4{0u, pc, 0u, ps} : u4{pc, 0u, ps, 0u};
        if (f == 0) A1(9, 0);
    }
    A1(10, 0);
    unsigned ya0, ya1, hxa, t2o, hr0, hr1, hpo;
    {
        int d0 = (64 * gq + 385 * ka) % DN;
        int d1 = d0 + 256; d1 = d1 >= DN ? d1 - DN : d1;
        if (gq == 3) d1 = DN + ka;
        ya0 = d0 * 8 + (d0 >> 3) * 16; ya1 = d1 * 8 + (d1 >> 3) * 16;
        hxa = lane * 128 + ((lane & 7) << 4) + wv * 4;
        A1(11, 0);
        t2o = (unsigned)(((8 * gq + (ka >> 3)) * 256 + s0) * 32 + ((((ka >> 1) & 3) ^ ((s0 >> 2) & 3)) * 8) + (ka & 1) * 4);
        const int n = tid >> 4, d = 32 * (n >> 1) + 8 * (col >> 2) + 4 * (n & 1) + (col & 3);
        hr0 = (d >> 3) * 128 + (((d & 7) ^ ((d >> 3) & 7)) << 4); hr1 = hr0 + 4096;
        hpo = n * 1024 + col * 16 + WS_HP + (s0 >> 2) * (28 * 1024) + (s0 & 3) * 256;
        PIN(ya0); PIN(ya1); PIN(hxa); PIN(t2o); PIN(hr0); PIN(hpo);
        (void)hr1;
    }
    PIN(cbr[0]); PIN(cbr[1]); PIN(cbr[2]); PIN(cbr[3]); PIN(cbi[0]); PIN(cbi[1]); PIN(cbi[2]); PIN(cbi[3]); PIN(ca3[0]); PIN(ca3[1]);
    N1(12, 4, 0);
    {
        const unsigned m = wave_max(poll(ws, 0, xcd, tid, tag));
        if (lane == 0) *(unsigned*)(scr + 26640 + wv * 4) = m;
    }
    __syncthreads();
    STAMP(5);
    unsigned wexp;
    {
        u4 tl[2], wl[2];
        const unsigned go = (unsigned)((tid >> 1) * 8192 + s0 * 32 + (tid & 1) * 16);
        LD16C(tl[0], go, ws + WS_T); LD16C(tl[1], go, ws + WS_T + 128 * 8192);
        LD16C(wl[0], go, ws + WS_WP); LD16C(wl[1], go, ws + WS_WP + 128 * 8192);
        N1(16, 4, 0);
        {
            const u4 m4 = *(const u4*)(scr + 26640);
            const unsigned m01 = m4.x > m4.y ? m4.x : m4.y, m23 = m4.z > m4.w ? m4.z : m4.w;
            wexp = (unsigned)__builtin_amdgcn_readfirstlane((int)((m01 > m23 ? m01 : m23) >> 7));
            wexp = wexp > 248u ? 248u : wexp;
        }
        const float wsc = __uint_as_float((250u - wexp) << 23);
        asm volatile("s_waitcnt vmcnt(4)" : "+v"(tl[0]), "+v"(tl[1]), "+v"(wl[0]), "+v"(wl[1]) :: "memory");
        STAMP(6);
        *(u4*)(scr + tid * 16) = tl[0]; *(u4*)(scr + 4096 + tid * 16) = tl[1];
        *(u4*)(scr + 8192 + tid * 16) = wl[0]; *(u4*)(scr + 12288 + tid * 16) = wl[1];
        A1(20, 0);
        __syncthreads();
        u2 tv[4], wq[4];
        {
            const char* p = scr + (64 * gq + col) * 32 + ((wv ^ (2 * ((col >> 3) & 1) + (gq & 1))) * 8);
#pragma unroll
            for (int e = 0; e < 4; ++e) tv[e] = *(const u2*)(p + e * 512);
#pragma unroll
            for (int e = 0; e < 4; ++e) wq[e] = *(const u2*)(p + 8192 + e * 512);
        }
        A1(21, 0);
        u4 d0, d1;
        d0 = u4{__builtin_amdgcn_perm(tv[1].x, tv[0].x, 0x05040100u), __builtin_amdgcn_perm(tv[3].x, tv[2].x, 0x05040100u),
                __builtin_amdgcn_perm(tv[1].x, tv[0].x, 0x07060302u), __builtin_amdgcn_perm(tv[3].x, tv[2].x, 0x07060302u)};
        d1 = u4{__builtin_amdgcn_perm(tv[1].y, tv[0].y, 0x05040100u), __builtin_amdgcn_perm(tv[3].y, tv[2].y, 0x05040100u),
                __builtin_amdgcn_perm(tv[1].y, tv[0].y, 0x07060302u), __builtin_amdgcn_perm(tv[3].y, tv[2].y, 0x07060302u)};
        f4 r0, i0, r1, i1;
        mf4<true>(r0, i0, r1, i1, d0, d1, cm[0], cm[1]);
        A1(22, 0);
        d0 = cmul_cvt(r0, i0, twc, -tws);
        A1(23, 0);
        d1 = cmul_cvt(r1, i1, twc, -tws);
        mf4<false>(r0, i0, r1, i1, d0, d1, cm[0], cm[1]);
        f4 wr0, wi0, wr1, wi1;
#pragma unroll
        for (int e = 0; e < 4; ++e) {
            wr0[e] = __uint_as_float(wq[e].x << 16); wi0[e] = __uint_as_float(wq[e].x & 0xffff0000u);
            wr1[e] = __uint_as_float(wq[e].y << 16); wi1[e] = __uint_as_float(wq[e].y & 0xffff0000u);
        }
        d0 = cmul_cvt(r0, i0, wr0 * wsc, wi0 * wsc); d1 = cmul_cvt(r1, i1, wr1 * wsc, wi1 * wsc);
        mf4<true>(r0, i0, r1, i1, d0, d1, cm[2], cm[3]);
        d0 = cmul_cvt(r0, i0, twc, tws); d1 = cmul_cvt(r1, i1, twc, tws);
        mf4<false>(r0, i0, r1, i1, d0, d1, cm[2], cm[3]);
        char* dst = scr + 16384 + (64 * gq + col) * 32 + ((wv ^ ((col >> 2) & 3)) * 8);
#pragma unroll
        for (int e = 0; e < 4; ++e) *(u2*)(dst + e * 512) = u2{pk_f16(cf{r0[e], i0[e]}), pk_f16(cf{r1[e], i1[e]})};
        __syncthreads();
        const u4 o0 = *(const u4*)(scr + 16384 + tid * 16), o1 = *(const u4*)(scr + 20480 + tid * 16);
        __builtin_amdgcn_raw_buffer_store_b128(o0, rs, tid * 16 + WS_T2 + s0 * 8192, 0, 16);
        __builtin_amdgcn_raw_buffer_store_b128(o1, rs, tid * 16 + WS_T2 + 4096 + s0 * 8192, 0, 16);
    }
    STAMP(7);
    asm volatile("s_waitcnt vmcnt(2)" ::: "memory");
    move_n<0, 0, 48>(traddr);
    LGKM0();
    DRAIN();
    __syncthreads();
    STAMP(8);
    publish(ws, 1, s0, tid, tag);
    if (b == 0 && tid == 0) __hip_atomic_store((GAS u64*)(ws + WS_NONCE), tag, RLX_AGENT);
    u2 bv[2]; unsigned bs[2];
    u4 ra, rb;
    {
        LD16(ra, xoff, x); LD16(rb, xoff, x + 256 * DN * 2);
        unsigned o = (unsigned)(nb1 * 64 + 16 * mb + 4 * gq) * 2u, ob = (unsigned)(nb2 * 64 + 16 * mb + col) * 2u;
        asm volatile("global_load_dwordx2 %0, %1, %2" : "=v"(bv[0]) : "v"(o), "s"(b1) : "memory");
        asm volatile("global_load_dwordx2 %0, %1, %2 offset:32" : "=v"(bv[1]) : "v"(o), "s"(b1) : "memory");
        asm volatile("global_load_ushort %0, %1, %2" : "=v"(bs[0]) : "v"(ob), "s"(b2) : "memory");
        asm volatile("global_load_ushort %0, %1, %2 offset:32" : "=v"(bs[1]) : "v"(ob), "s"(b2) : "memory");
    }
    N1(24, 8, -6);
    if (!has1) {
        w1w = make_srd(w2 + ((long long)(448 * wv + 32 * (31 - off)) - 32) * (SN * 2) + (xcd >> 1) * 1024, off >= 25 ? 63 * SN * 2 + 1024 : 0);
        row1 = SN * 2;
    }
    N1(32, 8, -6);
    poll(ws, 1, xcd, tid, tag);
    asm volatile("" : "+v"(ra), "+v"(rb), "+v"(bv[0]), "+v"(bv[1]), "+v"(bs[0]), "+v"(bs[1]) :: "memory");
    __syncthreads();
    STAMP(9);
    {
        unsigned tq[8];
#pragma unroll
        for (int qq = 0; qq < 8; ++qq)
            asm volatile("global_load_dword %0, %1, %2 sc1" : "=v"(tq[qq]) : "v"(t2o), "s"(ws + WS_T2 + qq * (32 * 8192)) : "memory");
        A1(40, -6); move_n<6, 0, 8>(traddr); A1(41, -6); move_n<7, 1, 8>(traddr); A1(42, -6); move_n<8, 2, 8>(traddr); A1(43, -6); move_n<9, 3, 8>(traddr);
        cf xv[8];
        A1(44, -6);
#pragma unroll
        for (int i = 0; i < 4; ++i) {
            xv[2 * i] = cf{__uint_as_float(ra[i] << 16), __uint_as_float(rb[i] << 16)};
            xv[2 * i + 1] = cf{__uint_as_float(ra[i] & 0xffff0000u), __uint_as_float(rb[i] & 0xffff0000u)};
            if (i == 1) A1(45, -6);
        }
        A1(46, -6);
        A1(47, -6);
        asm volatile("s_waitcnt vmcnt(8) lgkmcnt(0)" : "+v"(tq[0]), "+v"(tq[1]), "+v"(tq[2]), "+v"(tq[3]), "+v"(tq[4]), "+v"(tq[5]), "+v"(tq[6]), "+v"(tq[7]) :: "memory");
        STAMP(10);
        cf rr[8];
#pragma unroll
        for (int qq = 0; qq < 8; ++qq) rr[qq] = unpk_f16(tq[qq]);
        A1(48, -6);
        {
            cf tm[8];
#pragma unroll
            for (int qq = 1; qq < 8; ++qq) tm[qq] = rr[qq] * ts[qq].w.xx;
            SB();
#pragma unroll
            for (int qq = 1; qq < 8; ++qq) rr[qq] = cfma(rr[qq].yx, ts[qq].n, tm[qq]);
        }
        A1(49, -6);
        dft8<true>(rr);
        A1(50, -6);
        {
            char* dst = scr + gq * (4 * 288) + ka * 4;
            unsigned pz[8];
#pragma unroll
            for (int j = 0; j < 8; ++j) pz[j] = pk_f16(rr[j]);
            A1(51, -6);
#pragma unroll
            for (int j = 0; j < 8; ++j) *(unsigned*)(dst + (16 * (j >> 2) + (j & 3)) * 288) = pz[j];
        }
        A1(52, -6);
        __syncthreads();
        STAMP(11);
        u4 bop[2];
        {
            u4 a0[4], a1[4];
#pragma unroll
            for (int ks = 0; ks < 4; ++ks) a0[ks] = *(const u4*)(scr + col * 288 + ks * 64 + gq * 16);
#pragma unroll
            for (int ks = 0; ks < 4; ++ks) a1[ks] = *(const u4*)(scr + (16 + col) * 288 + ks * 64 + gq * 16);
            A1(53, -6);
            f4 re, im;
            mf8(re, im, a0, cbr, cbi);
            A1(54, -6);
            bop[0] = cvt8(re, im);
            mf8(re, im, a1, cbr, cbi);
            A1(55, -6);
            bop[1] = cvt8(re, im);
        }
        f4 y00, y01, y10, y11;
        mf4<false>(y00, y01, y10, y11, bop[0], bop[1], ca3[0], ca3[1]);
        A1(56, -14);
        char* yx = scr + 32 * 288;
        *(cf*)(yx + ya0) = y00.xy; *(cf*)(yx + ya0 + 5120) = y00.zw; *(cf*)(yx + ya0 + 10240) = y10.xy; *(cf*)(yx + ya0 + 15360) = y10.zw;
        *(cf*)(yx + ya1) = y01.xy; *(cf*)(yx + ya1 + 5120) = y01.zw; *(cf*)(yx + ya1 + 10240) = y11.xy; *(cf*)(yx + ya1 + 15360) = y11.zw;
        A1(57, -14);
        __syncthreads();
        STAMP(12);
        const float ysc = __uint_as_float((wexp + 2u) << 23) * (32.0f * 256.0f / ((float)SN * (float)DN));
        f4 yy[4];
#pragma unroll
        for (int i = 0; i < 4; ++i) yy[i] = *(const f4*)(yx + wv * 5120 + lc * 80 + i * 16);
        A1(58, -14);
        cf v[8];
#pragma unroll
        for (int i = 0; i < 4; ++i) {
            v[2 * i] = cfma(yy[i].xy, cf{ysc, ysc}, xv[2 * i]);
            v[2 * i + 1] = cfma(yy[i].zw, cf{ysc, ysc}, xv[2 * i + 1]);
        }
        A1(59, -14);
        u4 ha, hb;
#pragma unroll
        for (int i = 0; i < 4; ++i) { ha[i] = pk_bf16(cf{v[2 * i].x, v[2 * i + 1].x}); hb[i] = pk_bf16(cf{v[2 * i].y, v[2 * i + 1].y}); }
        A1(60, -14);
#pragma unroll
        for (int i = 0; i < 4; ++i) {
            v[2 * i] = cf{__uint_as_float(ha[i] << 16), __uint_as_float(hb[i] << 16)};
            v[2 * i + 1] = cf{__uint_as_float(ha[i] & 0xffff0000u), __uint_as_float(hb[i] & 0xffff0000u)};
            if (i == 1) A1(61, -14);
        }
        A1(62, -14);
        cf mean, var;
        row_stats<8, true>(v, lane < 56 ? 1.f : 0.f, mean, var);
        A1(63, -14);
        __builtin_amdgcn_raw_buffer_store_b128(ha, rs, xoff + WS_H, 0, 16);
        const cf r1 = rsq2(var + 1e-5f);
#pragma unroll
        for (int e = 0; e < 8; ++e) v[e] = v[e] - mean;
        const cf rr2 = r1 * rsq2(cfma(var * r1, r1, cf{1e-5f, 1e-5f}));
        SB();
        __builtin_amdgcn_raw_buffer_store_b128(hb, rs, xoff + (WS_H + 256 * DN * 2), 0, 16);
#pragma unroll
        for (int e = 0; e < 8; ++e) v[e] = v[e] * rr2;
        SB();
        char* hx = scr;
#pragma unroll
        for (int e = 0; e < 8; ++e) *(unsigned*)(hx + (hxa ^ (e << 4))) = pk_bf16(v[e]);
        __syncthreads();
        const u4 p0 = *(const u4*)(hx + hr0), p1 = *(const u4*)(hx + hr0 + 4096);
        __builtin_amdgcn_raw_buffer_store_b128(p0, rs, hpo, 0, 16);
        if (wv < 3) __builtin_amdgcn_raw_buffer_store_b128(p1, rs, hpo + 16 * 1024, 0, 16);
    }
    STAMP(13);
    asm volatile("s_waitcnt vmcnt(3)" ::: "memory");
    move_n<10, 4, 32>(traddr);
    move_n<14, 0, 16>(traddr);
    LGKM0();
    DRAIN();
    __syncthreads();
    STAMP(14);
    publish(ws, 2, s0, tid, tag);
    N2(0, 28, 0);
    const unsigned aoff0 = (unsigned)lane * 16u + (wv & 1) * 2048u, aoff1 = (unsigned)lane * 16u + (1 - (wv & 1)) * 2048u;
    f4 acc[4][4];
#define RED_W(c) do { char* dst_ = ((c) < 3 ? smem + LDS_SCR + (wv * 2 + (c) - 1) * 4096 : smem + wv * 32768 + 7 * 4096) + lane * 16; \
        _Pragma("unroll") for (int mi = 0; mi < 2; ++mi) _Pragma("unroll") for (int ni = 0; ni < 2; ++ni) \
            *(f4*)(dst_ + (mi * 2 + ni) * 1024) = acc[2 * ((c) >> 1) + mi][2 * ((c) & 1) + ni]; } while (0)
#define RED_R() do { f4 pr_[3][4]; \
        _Pragma("unroll") for (int c = 1; c < 4; ++c) { const int sw = wv ^ c; \
            const char* src_ = (c < 3 ? smem + LDS_SCR + (sw * 2 + c - 1) * 4096 : smem + sw * 32768 + 7 * 4096) + lane * 16; \
            _Pragma("unroll") for (int k = 0; k < 4; ++k) pr_[c - 1][k] = *(const f4*)(src_ + k * 1024); } \
        SB(); \
        _Pragma("unroll") for (int k = 0; k < 4; ++k) acc[k >> 1][k & 1] = (acc[k >> 1][k & 1] + pr_[0][k]) + (pr_[1][k] + pr_[2][k]); } while (0)
    if (has1) {
        poll(ws, 2, xcd, tid, tag);
        STAMP(15);
        gemm_loop<true, 16, GPD, 8, 0>(acc, ws + WS_HP + db1 * 4096 + (size_t)wv * (16 * 28 * 1024), aoff0, aoff1, traddr);
        STAMP(16);
        A2(28, -7); RED_W(1); A2(29, -7); RED_W(2); A2(30, -7); RED_W(3); A2(31, -7);
        __syncthreads();
        RED_R();
        A2(32, -7);
        cf vv[8], zz[8];
#pragma unroll
        for (int k = 0; k < 8; ++k) {
            const f4 a = acc[(k >> 1) & 1][k >> 2];
            vv[k] = ((k & 1) ? a.zw : a.xy) + unpk_bf16(bv[(k >> 1) & 1][k & 1]);
        }
        A2(33, -7);
        const float KA = -2.0f * 0.7978845608028654f * 1.4426950408889634f;
#pragma unroll
        for (int k = 0; k < 8; ++k) zz[k] = vv[k] * vv[k];
        SB();
#pragma unroll
        for (int k = 0; k < 8; ++k) zz[k] = cfma(zz[k], cf{0.044715f * KA, 0.044715f * KA}, cf{KA, KA});
        A2(34, -7);
#pragma unroll
        for (int k = 0; k < 8; ++k) zz[k] = zz[k] * vv[k];
        A2(35, -7);
#pragma unroll
        for (int k = 0; k < 8; ++k) zz[k] = cf{__builtin_amdgcn_exp2f(zz[k].x), __builtin_amdgcn_exp2f(zz[k].y)};
        SB();
#pragma unroll
        for (int k = 0; k < 8; ++k) zz[k] = zz[k] + 1.0f;
        SB();
#pragma unroll
        for (int k = 0; k < 8; ++k) zz[k] = cf{__builtin_amdgcn_rcpf(zz[k].x), __builtin_amdgcn_rcpf(zz[k].y)};
        SB();
#pragma unroll
        for (int k = 0; k < 8; ++k) vv[k] = vv[k] * zz[k];
        SB();
        const int ks = nb1 * 2 + (wv >> 1);
#pragma unroll
        for (int ni = 0; ni < 2; ++ni)
            __builtin_amdgcn_raw_buffer_store_b128(u4{pk_bf16(vv[4 * ni]), pk_bf16(vv[4 * ni + 1]), pk_bf16(vv[4 * ni + 2]), pk_bf16(vv[4 * ni + 3])}, rs,
                                                   lane * 16 + ni * 1024 + WS_UP + (ks * 28 + db1 * 4 + 2 * (wv & 1)) * 1024, 0, 16);
        STAMP(17);
    } else {
        if (off >= 25) {
            const u4 sq = make_srd(w2 + (size_t)(448 * wv + 224 + 32 * (off - 25)) * (SN * 2) + (xcd >> 1) * 1024, 31 * SN * 2 + 1024);
            const unsigned vp = lane * 16;
#pragma unroll 1
            for (int i = 0; i < 32; ++i) GLDS_B(sq, vp, (unsigned)(i * SN * 2), ldsbase + 28672);
        }
        DRAIN();
        if (!has2) return;
        move_n<0, 0, 56>(traddr);
        LGKM0();
        N2(28, 8, -7);
    }
    DRAIN();
    __syncthreads();
    STAMP(18);
    if (has1) publish(ws, 3, db1 * 28 + nb1, tid, tag);
    N2(36, 20, -7);
    poll(ws, 3, xcd, db2 * 28 + wv * 7 + (lane < 7 ? lane : 0), tag);
    STAMP(19);
    u4 hv[2];
    const unsigned ho = (unsigned)((nb2 * 64 + 16 * mb + col) * DN + db2 * 64 + 32 * (wv & 1) + 8 * gq) * 2u;
    asm volatile("global_load_dwordx4 %0, %1, %2 sc1" : "=v"(hv[0]) : "v"(ho), "s"(ws + WS_H) : "memory");
    asm volatile("global_load_dwordx4 %0, %1, %2 sc1" : "=v"(hv[1]) : "v"(ho), "s"(ws + WS_H + 16 * DN * 2) : "memory");
    gemm_loop<false, 14, GPD, 0, 7>(acc, ws + WS_UP + db2 * 4096 + (size_t)wv * (14 * 28 * 1024), aoff0, aoff1, traddr);
    STAMP(20);
    asm volatile("" : "+v"(hv[0]), "+v"(hv[1]));
    RED_W(1); RED_W(2); RED_W(3);
    cf hb2[8];
#pragma unroll
    for (int k = 0; k < 8; ++k) hb2[k] = unpk_bf16(hv[k >> 2][k & 3]) + __uint_as_float(bs[k >> 2] << 16);
    __syncthreads();
    RED_R();
#pragma unroll
    for (int mi = 0; mi < 2; ++mi) {
        const u4 o = u4{pk_bf16(acc[mi][0].xy + hb2[4 * mi]), pk_bf16(acc[mi][0].zw + hb2[4 * mi + 1]),
                        pk_bf16(acc[mi][1].xy + hb2[4 * mi + 2]), pk_bf16(acc[mi][1].zw + hb2[4 * mi + 3])};
        *(u4*)((char*)out + ho + mi * (16 * DN * 2)) = o;
    }
    asm volatile(".Lkf_code_end:" ::: "memory");
    STAMP(21);
    STAMP_FLUSH(0);
}

extern "C" void kernel_launch(void* const* d_in, const int* in_sizes, int n_in,
                              void* d_out, int out_size, void* d_ws, size_t ws_size,
                              hipStream_t stream) {
    (void)hipFuncSetAttribute((const void*)kf_fused, hipFuncAttributeMaxDynamicSharedMemorySize, LDS_TOTAL);
    kf_fused<<<256, 256, LDS_TOTAL, stream>>>((char*)d_ws, (const char*)d_in[0], (const char*)d_in[1], (const char*)d_in[2],
                                              (const char*)d_in[4], (const char*)d_in[5], (const char*)d_in[6], (const char*)d_in[7], (u16*)d_out);
}
```

```cpp
#include <hip/hip_runtime.h>
#include <hip/hip_bf16.h>
#include <stdint.h>

typedef unsigned short u16;
typedef float cf __attribute__((ext_vector_type(2)));
typedef float f4 __attribute__((ext_vector_type(4)));
typedef unsigned u4 __attribute__((ext_vector_type(4)));
typedef unsigned u2 __attribute__((ext_vector_type(2)));
typedef __bf16 bf2 __attribute__((ext_vector_type(2)));
typedef _Float16 h2 __attribute__((ext_vector_type(2)));

#define SN 2048
#define DN 448
#define DFQ 225
#define FN 1792

__device__ __forceinline__ cf cfma(cf a, cf b, cf c) { return __builtin_elementwise_fma(a, b, c); }
__device__ __forceinline__ cf addi(cf a, cf b) { return cfma(b.yx, cf{-1.f, 1.f}, a); }
__device__ __forceinline__ cf subi(cf a, cf b) { return cfma(b.yx, cf{1.f, -1.f}, a); }
struct Tw { cf w; cf n; };
__device__ __forceinline__ Tw mktw(cf w) { Tw t; t.w = w; t.n = cf{-w.y, w.y}; return t; }

template <bool INV> __device__ __forceinline__ cf rot_m(cf a, cf b) { return INV ? addi(a, b) : subi(a, b); }
template <bool INV> __device__ __forceinline__ cf rot_p(cf a, cf b) { return INV ? subi(a, b) : addi(a, b); }

template <bool INV> __device__ __forceinline__ void dft8(cf (&x)[8]) {
    const float R = 0.70710678118654752440f;
    cf a0 = x[0] + x[4], a4 = x[0] - x[4], a1 = x[1] + x[5], a5 = x[1] - x[5];
    cf a2 = x[2] + x[6], a6 = x[2] - x[6], a3 = x[3] + x[7], a7 = x[3] - x[7];
    cf b0 = a0 + a2, b2 = a0 - a2, b1 = a1 + a3, b3 = a1 - a3;
    cf c0 = rot_m<INV>(a4, a6), c2 = rot_p<INV>(a4, a6);
    cf e = rot_m<INV>(a5, a7), f = rot_p<INV>(a5, a7);
    cf g = rot_m<INV>(e, e), h = rot_m<INV>(f, f);
    x[0] = b0 + b1; x[4] = b0 - b1;
    x[2] = rot_m<INV>(b2, b3); x[6] = rot_p<INV>(b2, b3);
    x[1] = cfma(g, cf{R, R}, c0); x[5] = cfma(g, cf{-R, -R}, c0);
    x[3] = cfma(h.yx, INV ? cf{-R, R} : cf{R, -R}, c2);
    x[7] = cfma(h.yx, INV ? cf{R, -R} : cf{-R, R}, c2);
}
__device__ __forceinline__ unsigned pk_f16(cf v) { return __builtin_bit_cast(unsigned, __builtin_convertvector(v, h2)); }
__device__ __forceinline__ cf unpk_f16(unsigned p) { return __builtin_convertvector(__builtin_bit_cast(h2, p), cf); }
__device__ __forceinline__ unsigned pk_bf16(cf v) { return __builtin_bit_cast(unsigned, __builtin_convertvector(v, bf2)); }
__device__ __forceinline__ cf unpk_bf16(unsigned p) { return cf{__uint_as_float(p << 16), __uint_as_float(p & 0xffff0000u)}; }

template <int CTRL, int RM> __device__ __forceinline__ float dpp_add(float v) {
    int t = __builtin_amdgcn_update_dpp(0, __float_as_int(v), CTRL, RM, 0xf, true);
    return v + __int_as_float(t);
}
__device__ __forceinline__ float swap32_add(float a, float b) {
    auto r = __builtin_amdgcn_permlane32_swap(__float_as_uint(a), __float_as_uint(b), false, false);
    return __uint_as_float(r[0]) + __uint_as_float(r[1]);
}
__device__ __forceinline__ float swap16_add(float a, float b) {
    auto r = __builtin_amdgcn_permlane16_swap(__float_as_uint(a), __float_as_uint(b), false, false);
    return __uint_as_float(r[0]) + __uint_as_float(r[1]);
}
__device__ __forceinline__ float rdlane(float v, int l) { return __int_as_float(__builtin_amdgcn_readlane(__float_as_int(v), l)); }
__device__ __forceinline__ void wave_sum4(cf& s, cf& q) {
    float v = swap16_add(swap32_add(s.x, s.y), swap32_add(q.x, q.y));
    v = dpp_add<0x111, 0xf>(v); v = dpp_add<0x112, 0xf>(v); v = dpp_add<0x114, 0xf>(v); v = dpp_add<0x118, 0xf>(v);
    s = cf{rdlane(v, 15), rdlane(v, 47)};
    q = cf{rdlane(v, 31), rdlane(v, 63)};
}

template <int NE, bool MASK>
__device__ __forceinline__ void row_stats(const cf (&v)[NE], float act, cf& mean, cf& var) {
    const cf piv = cf{rdlane(v[0].x, 0), rdlane(v[0].y, 0)};
    cf d[NE];
#pragma unroll
    for (int e = 0; e < NE; ++e) d[e] = v[e] - piv;
    cf s = ((d[0] + d[1]) + (d[2] + d[3])) + ((d[4] + d[5]) + (NE == 8 ? d[6] + d[NE - 1] : d[6]));
    cf q0 = cfma(d[2], d[2], d[0] * d[0]), q1 = cfma(d[3], d[3], d[1] * d[1]);
    q0 = cfma(d[4], d[4], q0); q1 = cfma(d[5], d[5], q1);
    q0 = cfma(d[6], d[6], q0);
    if (NE == 8) q1 = cfma(d[NE - 1], d[NE - 1], q1);
    cf q = q0 + q1;
    if (MASK) { q = q * act; s = s * act; }
    wave_sum4(s, q);
    const cf md = s * (1.0f / DN);
    var = cfma(-md, md, q * (1.0f / DN));
    var = cf{__builtin_fmaxf(var.x, 0.f), __builtin_fmaxf(var.y, 0.f)};
    mean = piv + md;
}
__device__ __forceinline__ cf rsq2(cf v) { return cf{__builtin_amdgcn_rsqf(v.x), __builtin_amdgcn_rsqf(v.y)}; }
__device__ __forceinline__ float cosr(float rev) { return __builtin_amdgcn_cosf(rev); }
__device__ __forceinline__ float sinr(float rev) { return __builtin_amdgcn_sinf(rev); }
#define PIN(x) asm volatile("" : "+v"(x))
template <int CTRL, int RM> __device__ __forceinline__ unsigned dpp_max(unsigned v) {
    unsigned t = (unsigned)__builtin_amdgcn_update_dpp(0, (int)v, CTRL, RM, 0xf, true);
    return v > t ? v : t;
}
__device__ __forceinline__ unsigned wave_max(unsigned v) {
    v = dpp_max<0x111, 0xf>(v); v = dpp_max<0x112, 0xf>(v); v = dpp_max<0x114, 0xf>(v); v = dpp_max<0x118, 0xf>(v);
    v = dpp_max<0x142, 0xa>(v); v = dpp_max<0x143, 0xc>(v);
    return (unsigned)__builtin_amdgcn_readlane((int)v, 63);
}

#define WS_FLAGS 0
#define WS_NONCE (5 * 8 * 4096)
#define WS_T   (1 << 20)
#define WS_WP  (3 << 20)
#define WS_T2  (5 << 20)
#define WS_H   (7 << 20)
#define WS_HP  (9 << 20)
#define WS_UP  (11 << 20)
#define WS_END (13 << 20)

typedef unsigned long long u64;
#define GAS __attribute__((address_space(1)))
#define RLX_AGENT __ATOMIC_RELAXED, __HIP_MEMORY_SCOPE_AGENT
#define PF_LOAD(dst, base, off) asm volatile("global_load_dword %0, %1, %2" : "=v"(dst) : "v"(off), "s"(base) : "memory")
__device__ __forceinline__ int s0_of_block(int b) { return (b & ~31) | ((b & 7) << 2) | ((b >> 3) & 3); }
__device__ __forceinline__ u4 cvt8(f4 a, f4 b) { return u4{pk_f16(a.xy), pk_f16(a.zw), pk_f16(b.xy), pk_f16(b.zw)}; }
__device__ __forceinline__ u4 make_srd(const char* base, unsigned bytes) {
    return u4{(unsigned)(uintptr_t)base, (unsigned)((uintptr_t)base >> 32) & 0xffffu, bytes, 0x00020000u};
}
#define GLDS_F(srd, voff, soff, ldsaddr, FL) asm volatile("s_mov_b32 m0, %3\n\ts_nop 0\n\tbuffer_load_dwordx4 %0, %1, %2 offen" FL " lds" :: "v"(voff), "s"(srd), "s"(soff), "s"(ldsaddr) : "memory")
#define GLDS_W(srd, voff, soff, ldsaddr) GLDS_F(srd, voff, soff, ldsaddr, " nt")
#define GLDS_B(srd, voff, soff, ldsaddr) asm volatile("s_mov_b32 m0, %3\n\ts_nop 0\n\tbuffer_load_dwordx4 %0, %1, %2 offen nt lds\n\tbuffer_load_dword %0, %1, %2 offen sc1 lds" :: "v"(voff), "s"(srd), "s"(soff), "s"(ldsaddr) : "memory")
#define CODE_ADDR(dst, sym) do { unsigned long long pc_; int dl_; \
    asm volatile("s_getpc_b64 %0\n.Lpc_%=:\n\ts_mov_b32 %1, " sym "-.Lpc_%=" : "=s"(pc_), "=s"(dl_)); dst = (const char*)(pc_ + (long long)dl_); } while (0)
#define GPD 8
#define GPD2 6
#define HK2 10
#define NP2 4
#define PSL 6
#define ISL 80
#define POSA 16
#define NAE 24
#define POSB 41
#define POSC 26
#define ISL2 30
#define PPOS 51
#define XB0 1
#define HKB 0
#define HK1 5
#define LDS_SCR 131072
#define LDS_TOTAL 163840
#define LD16(dst, voff, base) asm volatile("global_load_dwordx4 %0, %1, %2" : "=v"(dst) : "v"(voff), "s"(base) : "memory")
#define LD16C(dst, voff, base) asm volatile("global_load_dwordx4 %0, %1, %2 sc1" : "=v"(dst) : "v"(voff), "s"(base) : "memory")
#define DRAIN() asm volatile("s_waitcnt vmcnt(0)" ::: "memory")

template <bool G1, int N, int SL>
__device__ __forceinline__ void stage1(u4 srd, unsigned voff0, unsigned voff1, unsigned ldsbase) {
    constexpr int t = N >> 2, i = N & 3;
    constexpr int goff = G1 ? (4 * t + i) * (FN * 2) : (32 * t + 4 * i) * (SN * 2);
    unsigned tmp;
    asm volatile("s_add_i32 m0, %2, %4\n\ts_mov_b32 %0, %5\n\tbuffer_load_dwordx4 %3, %1, %0 offen lds"
                 : "=&s"(tmp) : "s"(srd), "s"(ldsbase), "v"((i & 1) ? voff1 : voff0), "i"(SL * 4096 + i * 1024), "i"(goff) : "memory", "scc");
}
template <bool G1, int N0, int CNT, int SLOFF>
__device__ __forceinline__ void stage_n(u4 srd, unsigned voff0, unsigned voff1, unsigned ldsbase) {
    if constexpr (CNT > 0) {
        stage1<G1, N0, (N0 >> 2) + SLOFF>(srd, voff0, voff1, ldsbase);
        stage_n<G1, N0 + 1, CNT - 1, SLOFF>(srd, voff0, voff1, ldsbase);
    }
}
__device__ __forceinline__ f4 fma4(f4 a, f4 b, f4 c) { return __builtin_elementwise_fma(a, b, c); }
__device__ __forceinline__ u4 cmul_cvt(f4 re, f4 im, f4 tr, f4 ti) { return cvt8(fma4(im, -ti, re * tr), fma4(re, ti, im * tr)); }
template <int A, int SL, int R>
__device__ __forceinline__ void move1(const unsigned (&traddr)[4]) {
    asm volatile("ds_read_b64_tr_b16 a[%c1:%c2], %0 offset:%c3" ::"v"(traddr[R >> 1]), "i"(16 * A + 2 * R), "i"(16 * A + 2 * R + 1),
                 "i"(SL * 4096 + (R & 1) * 512) : "memory");
}
template <int A0, int SL0, int CNT>
__device__ __forceinline__ void move_n(const unsigned (&traddr)[4]) {
    if constexpr (CNT > 0) {
        move_n<A0, SL0, CNT - 1>(traddr);
        move1<A0 + ((CNT - 1) >> 3), SL0 + ((CNT - 1) >> 3), (CNT - 1) & 7>(traddr);
    }
}
#define LGKM0() asm volatile("s_waitcnt lgkmcnt(0)" ::: "memory")
__device__ __forceinline__ void publish(char* ws, int seam, int idx, int tid, u64 word) {
    if (tid < 8) __hip_atomic_store((GAS u64*)(ws + WS_FLAGS + (seam * 8 + tid) * 4096 + idx * 16), word, RLX_AGENT);
}
__device__ __forceinline__ unsigned poll(char* ws, int seam, int rep, int idx, u64 tag) {
    GAS u64* p = (GAS u64*)(ws + WS_FLAGS + (seam * 8 + rep) * 4096 + idx * 16);
    u64 v = 0;
    for (int spin = 0; spin < (1 << 16); ++spin) {
        v = __hip_atomic_load(p, RLX_AGENT);
        if (__all(((v ^ tag) >> 16) == 0ull)) break;
        __builtin_amdgcn_s_sleep(PSL);
    }
    __builtin_amdgcn_fence(__ATOMIC_ACQUIRE, "wavefront");
    return (unsigned)v & 0xffffu;
}

#define POLL_ISSUE(dst, voff, seam) asm volatile("global_load_dwordx2 %0, %1, %2 sc1" : "=v"(dst) : "v"(voff), "s"(ws + WS_FLAGS + (seam) * 8 * 4096) : "memory")
__device__ __forceinline__ void poll_issue(u2& dst, unsigned voff, char* ws, int seam) { POLL_ISSUE(dst, voff, seam); }
__device__ __forceinline__ bool poll_ok(u2 v, u64 tag) { return __all((((((u64)v.y << 32) | v.x) ^ tag) >> 16) == 0ull); }
#define ACT_LD(dst, voff, base) asm volatile("global_load_dwordx4 %0, %1, %2 sc1" : "=v"(dst) : "v"(voff), "s"(base) : "memory")
template <int NK> __device__ __forceinline__ const char* act_ptr(const char* abase, int G) { return abase + (size_t)(G % NK) * (28 * 1024) + (G / NK) * 2048; }
template <int NK, int G, int CNT>
__device__ __forceinline__ void act_issue(u4 (&bfr)[2 * NK][2], const char* abase, unsigned ao0, unsigned ao1) {
    if constexpr (CNT > 0) {
        ACT_LD(bfr[G][0], ao0, act_ptr<NK>(abase, G)); ACT_LD(bfr[G][1], ao1, act_ptr<NK>(abase, G));
        act_issue<NK, G + 1, CNT - 1>(bfr, abase, ao0, ao1);
    }
}
template <int V> struct IC { static constexpr int value = V; };
template <bool G1, int NK, int PD, int G0, int GEND, int EXTRA, int MT0, int MN, int MA0, int HK, bool TAIL, int T, int I, int XT = 0, int HKX = -1, typename F, typename F2>
__device__ __forceinline__ void gemm_mfma(f4 (&acc)[4][2], u4 (&bfr)[2 * NK][2], const char* abase, unsigned ao0, unsigned ao1,
                                          const unsigned (&traddr)[4], F&& hook, F2&& tail) {
    constexpr int n = I >> 2, m = I & 3, G = G0 + T;
    if constexpr (I == 0) {
        constexpr int last = (TAIL || G + PD - 1 < GEND - 1) ? G + PD - 1 : GEND - 1;
        asm volatile("s_waitcnt vmcnt(%2)" : "+v"(bfr[G][0]), "+v"(bfr[G][1]) : "i"((last - G) * 2 + (T < PD ? EXTRA : 0) + (T > HK ? XT : 0)) : "memory");
        if constexpr (MN > 0 && T == MT0 + MN) LGKM0();
        if constexpr (T > MT0 + 1 && T < MT0 + MN) asm volatile("s_waitcnt lgkmcnt(8)" ::: "memory");
        if constexpr (T == HK) hook();
        if constexpr (T == HKX) tail(IC<0>{});
    }
    if constexpr (T == 0) {
        if (G1) asm volatile("v_mfma_f32_16x16x32_bf16 %0, a[%c2:%c3], %1, 0" : "=&v"(acc[m][n]) : "v"(bfr[G][n]), "i"(16 * T + 4 * m), "i"(16 * T + 4 * m + 3));
        else asm volatile("v_mfma_f32_16x16x32_bf16 %0, %1, a[%c2:%c3], 0" : "=&v"(acc[m][n]) : "v"(bfr[G][n]), "i"(16 * T + 4 * m), "i"(16 * T + 4 * m + 3));
    } else {
        if (G1) asm volatile("v_mfma_f32_16x16x32_bf16 %0, a[%c2:%c3], %1, %0" : "+v"(acc[m][n]) : "v"(bfr[G][n]), "i"(16 * T + 4 * m), "i"(16 * T + 4 * m + 3));
        else asm volatile("v_mfma_f32_16x16x32_bf16 %0, %1, a[%c2:%c3], %0" : "+v"(acc[m][n]) : "v"(bfr[G][n]), "i"(16 * T + 4 * m), "i"(16 * T + 4 * m + 3));
    }
    if constexpr (m == 1 && G + PD < GEND) ACT_LD(bfr[G + PD][n], n ? ao1 : ao0, act_ptr<NK>(abase, G + PD));
    if constexpr (m == 1 && G + PD >= GEND && TAIL) tail(IC<2 * (G + PD - GEND) + n>{});
    if constexpr (T >= MT0 && T < MT0 + MN) move1<MA0 + T - MT0, (MA0 + T - MT0) & 7, I>(traddr);
    if constexpr (I < 7) gemm_mfma<G1, NK, PD, G0, GEND, EXTRA, MT0, MN, MA0, HK, TAIL, T, I + 1, XT, HKX>(acc, bfr, abase, ao0, ao1, traddr, hook, tail);
    else if constexpr (T + 1 < NK) gemm_mfma<G1, NK, PD, G0, GEND, EXTRA, MT0, MN, MA0, HK, TAIL, T + 1, 0, XT, HKX>(acc, bfr, abase, ao0, ao1, traddr, hook, tail);
    else asm volatile("s_nop 7\n\ts_nop 1" : "+v"(acc[0][0]), "+v"(acc[0][1]), "+v"(acc[1][0]), "+v"(acc[1][1]), "+v"(acc[2][0]), "+v"(acc[2][1]), "+v"(acc[3][0]), "+v"(acc[3][1]));
}

#define SB() __builtin_amdgcn_sched_barrier(0)
#define A1(N, SLOFF) do { SB(); stage1<true, N, ((N) >> 2) + (SLOFF)>(w1w, v1o[0], v1o[1], ldsbase); SB(); } while (0)
#define A2(N, SLOFF) do { SB(); stage1<false, N, ((N) >> 2) + (SLOFF)>(w2w, v2o[0], v2o[1], ldsbase); SB(); } while (0)
#define N1(N0, CNT, SLOFF) stage_n<true, N0, CNT, SLOFF>(w1w, v1o[0], v1o[1], ldsbase)
#define N2(N0, CNT, SLOFF) stage_n<false, N0, CNT, SLOFF>(w2w, v2o[0], v2o[1], ldsbase)
#define PFW(srd, i) do { SB(); GLDS_B(srd, vpf, (unsigned)((2 * ((i) & 3) + 32 * ((i) >> 2)) * FN * 2), dump); SB(); } while (0)

extern "C" __global__ __launch_bounds__(256, 1) __attribute__((amdgpu_waves_per_eu(1, 1))) void kf_fused(
        char* __restrict__ ws, const char* __restrict__ x, const char* __restrict__ wr, const char* __restrict__ wi,
        const char* __restrict__ w1, const char* __restrict__ b1, const char* __restrict__ w2, const char* __restrict__ b2,
        u16* __restrict__ out) {
    extern __shared__ __attribute__((aligned(16))) char smem[];
    asm volatile("" ::: "a0", "a255");
    const int tid = threadIdx.x, lane = tid & 63, b = blockIdx.x;
    const int wv = __builtin_amdgcn_readfirstlane(tid >> 6);
    const int xcd = b & 7, off = b >> 3, s0 = s0_of_block(b);
    char* scr = smem + LDS_SCR;
    const unsigned ldsbase = (unsigned)(uintptr_t)smem + wv * 32768;
    const unsigned dump = (unsigned)(uintptr_t)smem + LDS_SCR + 28672 + wv * 1024;
    const int gq = lane >> 4, col = lane & 15, ka = 16 * wv + col;
    unsigned pfc;
    {
        const char* code; CODE_ADDR(code, "kf_fused");
        unsigned clen;
        asm volatile("s_mov_b32 %0, .Lkf_code_end-kf_fused" : "=s"(clen));
        const unsigned cl = (unsigned)((off & 7) * 32 + wv * 8 + (lane & 7)) * 128u;
        const unsigned oc = cl < clen ? cl : 0u;
        PF_LOAD(pfc, code, oc);
    }
    cf v7[7];
    u2 nonce;
    {
        const char* xa = x + (size_t)(s0 + 512 * wv) * (DN * 2);
        const unsigned o = lane * 2;
#pragma unroll
        for (int e = 0; e < 7; ++e) {
            float a = 0.f, c = 0.f;
            asm volatile("global_load_short_d16_hi %0, %1, %2 offset:%3 nt" : "+v"(a) : "v"(o), "s"(xa), "i"(128 * e) : "memory");
            asm volatile("global_load_short_d16_hi %0, %1, %2 offset:%3 nt" : "+v"(c) : "v"(o), "s"(xa + 256 * DN * 2), "i"(128 * e) : "memory");
            v7[e] = cf{a, c};
        }
        const unsigned z = 0;
        asm volatile("global_load_dwordx2 %0, %1, %2 sc1" : "=v"(nonce) : "v"(z), "s"(ws + WS_NONCE) : "memory");
        SB();
    }
    const auto rs = __builtin_amdgcn_make_buffer_rsrc(ws, 0, WS_END, 0x00020000);
    const int hs0 = 2 * ((s0 >> 3) & 1) + ((s0 >> 6) & 1);
    const unsigned vpf = (unsigned)(64 * off + 8 * wv + (lane >> 5)) * (FN * 2) + (7 * (xcd >> 1) + 3 * (xcd & 1)) * 128 + (lane & 31) * 16;
    const unsigned sgk = (ka & 1) ? 0x80008000u : 0u;
    Tw ts[8];
    float tsc, tss;
    u64 tag;
    u4 clr;
    {
        const u4 srdw = make_srd((wv & 1) ? wi : wr, SN * DFQ * 2);
        const u4 s1 = make_srd(w1, SN * FN * 2);
        const unsigned wso = (unsigned)(((wv >> 1) ? 255 - s0 : s0) * 3600), vo16 = lane * 16;
#define PFE(i) PFW(s1, i)
#define WROW(i) do { SB(); GLDS_W(srdw, vo16, wso + (i) * 1024, ldsbase + 28672 + (i) * 1024); SB(); } while (0)
        u4 cb[2][2], ca[4];
#define S1C(jp) do { const int nu = 8 * gq + 2 * (jp); \
            const float r0 = (float)((nu * ka) & 63) * (1.f / 64), r1 = (float)(((nu + 1) * ka) & 63) * (1.f / 64); \
            cb[0][0][jp] = pk_f16(cf{cosr(r0), cosr(r1)}); cb[0][1][jp] = cb[0][0][jp] ^ sgk; \
            cb[1][0][jp] = pk_f16(cf{-sinr(r0), -sinr(r1)}); cb[1][1][jp] = cb[1][0][jp] ^ sgk; } while (0)
        WROW(0); S1C(0); WROW(1); S1C(1); WROW(2); S1C(2); WROW(3); S1C(3);
        PFE(0);
        {
            const int ko = col >> 2, jj = (col >> 1) & 1, outp = col & 1;
#pragma unroll
            for (int hi = 0; hi < 2; ++hi) {
                const int n1 = 4 * hi + gq;
                const float rv = (float)(n1 * ko) * (1.f / 7), ok = n1 < 7 ? 1.f : 0.f;
                const float c = cosr(rv) * ok, sn = sinr(rv) * ok;
                const unsigned pp = pk_f16(cf{outp ? -sn : c, outp ? c : sn});
                const unsigned pre = jj ? pp << 16 : pp & 0xffffu, pim = jj ? pp & 0xffff0000u : pp >> 16;
                ca[0][2 * hi] = pre; ca[0][2 * hi + 1] = 0u; ca[1][2 * hi] = pim; ca[1][2 * hi + 1] = 0u;
                ca[2][2 * hi] = 0u; ca[2][2 * hi + 1] = pre; ca[3][2 * hi] = 0u; ca[3][2 * hi + 1] = pim;
                if (hi == 0) PFE(1); else PFE(2);
            }
        }
        {
            const float rv = (float)(s0 * (lane & 7)) * (1.f / 2048);
            tsc = cosr(rv); tss = sinr(rv);
#pragma unroll
            for (int q = 1; q < 8; ++q) ts[q] = mktw(cf{rdlane(tsc, q), rdlane(tss, q)});
        }
        PFE(3);
        unsigned wa[7];
        {
            const unsigned base = (32 * (wv >> 1) + 2 * (wv & 1)) * 160 + lane * 2, r7 = (unsigned)(lane % 7) * 640u;
#pragma unroll
            for (int e = 0; e < 7; ++e) {
                const unsigned u = r7 + e * 640u, u2_ = u - 4480u;
                wa[e] = base + (u < u2_ ? u : u2_);
                PIN(wa[e]);
            }
        }
        PFE(4);
        unsigned ga[2][4], gs[4];
        {
            const int G = lane >> 1;
            const int kb = 64 * (G >> 3) + 56 * (G & 7);
            const unsigned mo0 = (wv == 0 && s0 == 0) ? 28672u : 28672u + 65536u + (8 - wv) * 450u, mo1 = 28672u + 65536u + (4 - wv) * 450u;
            const unsigned do0 = 28672u + wv * 450u, do1 = do0 + 1800u;
#pragma unroll
            for (int j = 0; j < 4; ++j) {
                int kd = kb + 14 * ((2 * (lane & 1) + (j >> 1)) ^ hs0) + 7 * (j & 1); kd = kd >= DN ? kd - DN : kd;
                const bool mir = kd > 224;
                const unsigned cb2 = (unsigned)(mir ? DN - kd : kd) * 2u;
                gs[j] = mir ? 0x80000000u : 0u;
                ga[0][j] = cb2 + (mir ? mo0 : do0); ga[1][j] = cb2 + (mir ? mo1 : do1);
                PIN(ga[0][j]); PIN(ga[1][j]);
                if (j == 0) PFE(5); if (j == 2) PFE(6);
            }
        }
        unsigned tia = 16384 + (8 * gq + (ka >> 3)) * 32 + ((((ka >> 1) & 3) ^ hs0) * 8) + (ka & 1) * 4;
        PIN(tia);
        PFE(7);
        *(unsigned*)(scr + (28 + 32 * (tid >> 7) + ((tid >> 5) & 3)) * 160 + (tid & 31) * 4) = 0u;
        SB();
        asm volatile("s_waitcnt vmcnt(%8)" : "+v"(pfc), "+v"(v7[0]), "+v"(v7[1]), "+v"(v7[2]), "+v"(v7[3]), "+v"(v7[4]), "+v"(v7[5]), "+v"(v7[6]) : "i"(5 + 16) : "memory");
        SB();
        {
            cf mean, var;
            row_stats<7, false>(v7, 1.f, mean, var);
            const cf rinv = rsq2(var + 1e-5f);
            SB();
            unsigned hz[7];
#pragma unroll
            for (int e = 0; e < 7; ++e) v7[e] = (v7[e] - mean) * rinv;
            SB();
#pragma unroll
            for (int e = 0; e < 7; ++e) hz[e] = pk_f16(v7[e]);
            SB();
#pragma unroll
            for (int e = 0; e < 7; ++e) { *(u16*)(scr + wa[e]) = (u16)hz[e]; *(u16*)(scr + wa[e] + 160) = (u16)(hz[e] >> 16); }
            SB();
        }
        asm volatile("s_waitcnt vmcnt(%1)" : "+v"(nonce) : "i"(16) : "memory");
        __syncthreads();
        u4 a[4][2];
#pragma unroll
        for (int rb = 0; rb < 4; ++rb) {
            const char* ap = scr + (16 * rb + col) * 160 + gq * 16;
            a[rb][0] = *(const u4*)ap; a[rb][1] = *(const u4*)(ap + 64);
        }
        SB();
        unsigned gr[2][4], gi[2][4];
        f4 c1[4][2];
#define MZ(d, x, y) do { asm volatile("v_mfma_f32_16x16x32_f16 %0, %1, %2, 0" : "=&v"(d) : "v"(x), "v"(y)); SB(); } while (0)
#define MA(d, x, y) do { asm volatile("v_mfma_f32_16x16x32_f16 %0, %1, %2, %0" : "+v"(d) : "v"(x), "v"(y)); SB(); } while (0)
#define GRD(i, j) do { gr[i][j] = *(const u16*)(smem + ga[i][j]); gi[i][j] = *(const u16*)(smem + ga[i][j] + 32768); SB(); } while (0)
        MZ(c1[0][0], a[0][0], cb[0][0]); GRD(0, 0); MZ(c1[0][1], a[0][0], cb[1][0]); GRD(0, 1);
        MZ(c1[1][0], a[1][0], cb[0][0]); GRD(0, 2); MZ(c1[1][1], a[1][0], cb[1][0]); GRD(0, 3);
        MA(c1[0][0], a[0][1], cb[0][1]); GRD(1, 0); MA(c1[0][1], a[0][1], cb[1][1]); GRD(1, 1);
        MA(c1[1][0], a[1][1], cb[0][1]); GRD(1, 2); MA(c1[1][1], a[1][1], cb[1][1]); GRD(1, 3);
        typedef unsigned short us2 __attribute__((ext_vector_type(2)));
        u4 wo[2];
        unsigned wp[2][4];
#define GCB(i, j) do { wp[i][j] = gr[i][j] | (gi[i][j] << 16); wo[i][j] = wp[i][j] ^ gs[j]; SB(); } while (0)
        MZ(c1[2][0], a[2][0], cb[0][0]); SB(); MZ(c1[2][1], a[2][0], cb[1][0]); SB();
        MZ(c1[3][0], a[3][0], cb[0][0]); GCB(0, 0); MZ(c1[3][1], a[3][0], cb[1][0]); GCB(0, 1);
        MA(c1[2][0], a[2][1], cb[0][1]); GCB(0, 2); MA(c1[2][1], a[2][1], cb[1][1]); GCB(0, 3);
        MA(c1[3][0], a[3][1], cb[0][1]);
        __builtin_amdgcn_raw_buffer_store_b128(wo[0], rs, tid * 16 + WS_WP + s0 * 8192, 0, 16);
        SB();
        MA(c1[3][1], a[3][1], cb[1][1]);
        const u4 bre0 = cvt8(c1[0][0], c1[1][0]), bim0 = cvt8(c1[0][1], c1[1][1]);
        SB();
        asm volatile("s_nop 0");
        const u4 bre1 = cvt8(c1[2][0], c1[3][0]), bim1 = cvt8(c1[2][1], c1[3][1]);
        SB();
        f4 o0, o1, o2, o3;
        MZ(o0, ca[0], bre0); GCB(1, 0); MZ(o1, ca[2], bre0); GCB(1, 1);
        MZ(o2, ca[0], bre1); GCB(1, 2); MZ(o3, ca[2], bre1); GCB(1, 3);
        MA(o0, ca[1], bim0);
        __builtin_amdgcn_raw_buffer_store_b128(wo[1], rs, tid * 16 + 4096 + WS_WP + s0 * 8192, 0, 16);
        SB();
        MA(o1, ca[3], bim0); MA(o2, ca[1], bim1); MA(o3, ca[3], bim1);
        asm volatile("s_nop 7" : "+v"(o0), "+v"(o1), "+v"(o2), "+v"(o3));
        cf r[8] = {o0.xy, o0.zw, o1.xy, o1.zw, o2.xy, o2.zw, o3.xy, o3.zw};
        dft8<false>(r);
        SB();
        {
            cf tm[8];
#pragma unroll
            for (int q = 1; q < 8; ++q) tm[q] = r[q] * ts[q].w.xx;
            SB();
#pragma unroll
            for (int q = 1; q < 8; ++q) r[q] = cfma(r[q].yx, ts[q].n.yx, tm[q]);
        }
        SB();
#pragma unroll
        for (int q = 0; q < 8; ++q) *(unsigned*)(scr + tia + q * 1024) = pk_f16(r[q]);
        __syncthreads();
        const u4 t0 = *(const u4*)(scr + 16384 + tid * 16), t1 = *(const u4*)(scr + 20480 + tid * 16);
        __builtin_amdgcn_raw_buffer_store_b128(t0, rs, tid * 16 + WS_T + s0 * 8192, 0, 16);
        __builtin_amdgcn_raw_buffer_store_b128(t1, rs, tid * 16 + 4096 + WS_T + s0 * 8192, 0, 16);
        SB();
        {
            const u64 n = ((u64)(unsigned)__builtin_amdgcn_readfirstlane((int)nonce.y) << 32) | (unsigned)__builtin_amdgcn_readfirstlane((int)nonce.x);
            tag = ((n >> 16) * 0x5DEECE66Dull + 0xB16B00B5F00Dull) << 16;
        }
        clr = u4{(unsigned)tag ^ 0x20000u, (unsigned)(tag >> 32), (unsigned)tag ^ 0x20000u, (unsigned)(tag >> 32)};
        if (tid < 32) __builtin_amdgcn_raw_buffer_store_b128(clr, rs, WS_FLAGS + 8 * 4096 + b * 512 + tid * 16, 0, 16);
        SB();
        {
            const u4 s2 = make_srd(w2, SN * FN * 2);
            const unsigned vp2 = (unsigned)(448 * wv + 2 * NP2 * off + (lane >> 5)) * (SN * 2) + xcd * 512 + (lane & 31) * 16;
#pragma unroll
            for (int i = 0; i < NP2; ++i) GLDS_B(s2, vp2, (unsigned)(i * 2 * SN * 2), dump);
        }
        us2 mx = {0, 0};
#pragma unroll
        for (int k = 0; k < 8; ++k) mx = __builtin_elementwise_max(mx, __builtin_bit_cast(us2, wp[k >> 2][k & 3] & 0x7fff7fffu));
        const unsigned wm = wave_max(mx.x > mx.y ? mx.x : mx.y);
        if (lane == 0) *(unsigned*)(scr + 26624 + wv * 4) = wm;
    }
    const int dh = xcd & 1;
    const bool has1 = off < 25 - dh, has2 = off < 28;
    const int id1 = has1 ? 25 * dh + off : 0, id2 = has2 ? off : 0;
    const int db1 = id1 % 7, db2 = id2 % 7;
    const int nb1 = 7 * (xcd >> 1) + id1 / 7, nb2 = 4 * xcd + id2 / 7;
    const int lc = lane < 56 ? lane : 55;
    const unsigned xoff = (unsigned)((s0 + 512 * wv) * DN + lc * 8) * 2u;
    const int mb = 2 * (wv >> 1);
    unsigned v1o[2], v2o[2], traddr[4], traddr2[4];
    {
        const int j = lane >> 3, cs = lane & 7;
#pragma unroll
        for (int par = 0; par < 2; ++par) {
            const int c = cs ^ (2 * (((j >> 1) & 1) | (par << 1)));
            v1o[par] = (unsigned)(256 * j * FN + nb1 * 64 + 8 * c) * 2u;
            PIN(v1o[par]);
        }
    }
    u4 w1w = make_srd(w1 + (size_t)wv * (64 * FN * 2), (unsigned)(SN * FN * 2 - wv * (64 * FN * 2)));
    if (!has1) w1w[2] = 0u;
    const u4 w2w = make_srd(w2 + (size_t)wv * (448 * SN * 2), has2 ? (unsigned)(SN * FN * 2 - wv * (448 * SN * 2)) : 0u);
    asm volatile("s_waitcnt vmcnt(%0)" :: "i"(2 * NP2) : "memory");
    __syncthreads();
    {
        const u4 m4 = *(const u4*)(scr + 26624);
        const unsigned m01 = m4.x > m4.y ? m4.x : m4.y, m23 = m4.z > m4.w ? m4.z : m4.w;
        publish(ws, 0, s0, tid, tag | (m01 > m23 ? m01 : m23));
    }
    u2 pv;
    const int pia = 32 * wv + (lane & 31) + 128 * (lane >> 5);
    const unsigned fa = (unsigned)(xcd * 4096 + pia * 16);
#define PA(k) do { if ((k) == POSA) POLL_ISSUE(pv, fa, 0); if ((k) < NAE) A1(k, 0); } while (0)
    u4 cm[4];
    f4 twc, tws;
    {
        float c16[4], s16[4];
#define S2C(e) do { const int n = col * (4 * gq + (e)); const float r16 = (float)(n & 15) * (1.f / 16), r256 = (float)n * (1.f / 256); \
            c16[e] = cosr(r16); s16[e] = sinr(r16); twc[e] = cosr(r256) * 0.0625f; tws[e] = sinr(r256) * 0.0625f; } while (0)
        PA(0); S2C(0); PA(1); S2C(1); PA(2); S2C(2); PA(3); S2C(3); PA(4);
        const unsigned c01 = pk_f16(cf{c16[0], c16[1]}), c23 = pk_f16(cf{c16[2], c16[3]});
        const unsigned s01 = pk_f16(cf{s16[0], s16[1]}), s23 = pk_f16(cf{s16[2], s16[3]});
        const unsigned n01 = s01 ^ 0x80008000u, n23 = s23 ^ 0x80008000u;
        cm[0] = u4{c01, c23, s01, s23}; cm[1] = u4{n01, n23, c01, c23};
        cm[2] = u4{c01, c23, n01, n23}; cm[3] = u4{s01, s23, c01, c23};
        PIN(cm[0]); PIN(cm[1]); PIN(cm[2]); PIN(cm[3]); PIN(twc); PIN(tws);
    }
    PA(5);
    {
        const int j = lane >> 3, cs = lane & 7;
#pragma unroll
        for (int par = 0; par < 2; ++par) {
            const int c = cs ^ (2 * (((j >> 1) & 1) | (par << 1)));
            v2o[par] = (unsigned)((16 * (j >> 2) + (j & 3)) * SN + nb2 * 64 + 8 * c) * 2u;
            PIN(v2o[par]);
        }
        PA(6);
        const int qq = (lane >> 2) & 3, pp = lane & 3;
        const int phi = ((qq >> 1) & 1) | ((gq & 1) << 1);
#pragma unroll
        for (int m = 0; m < 4; ++m) {
            traddr[m] = ldsbase + (8 * gq + qq) * 128 + (((m ^ mb) ^ phi) * 32) + pp * 8; PIN(traddr[m]);
            traddr2[m] = ldsbase + (8 * gq + qq) * 128 + (((m ^ wv) ^ phi) * 32) + pp * 8; PIN(traddr2[m]);
        }
    }
    PA(7); PA(8); PA(9); PA(10); PA(11);
    PA(12); PA(13); PA(14); PA(15);
    PA(16); PA(17); PA(18); PA(19); PA(20); PA(21); PA(22); PA(23);
    asm volatile("s_waitcnt vmcnt(%1)" : "+v"(pv) : "i"(NAE - POSA) : "memory");
    unsigned wpay = pv.x & 0xffffu;
    if (!poll_ok(pv, tag)) wpay = poll(ws, 0, xcd, pia, tag);
    unsigned wexp;
    {
        u4 tl[2], wl[2];
        const unsigned go = (unsigned)((tid >> 1) * 8192 + s0 * 32 + (tid & 1) * 16);
        LD16C(tl[0], go, ws + WS_T); LD16C(tl[1], go, ws + WS_T + 128 * 8192);
        LD16C(wl[0], go, ws + WS_WP); LD16C(wl[1], go, ws + WS_WP + 128 * 8192);
        N1(NAE, 24 - NAE, 0);
        {
            const unsigned m = wave_max(wpay);
            if (lane == 0) *(unsigned*)(scr + 26640 + wv * 4) = m;
        }
        asm volatile("s_waitcnt vmcnt(%2)" : "+v"(tl[0]), "+v"(tl[1]) : "i"(2 + 24 - NAE) : "memory");
        *(u4*)(scr + tid * 16) = tl[0]; *(u4*)(scr + 4096 + tid * 16) = tl[1];
        SB();
        __syncthreads();
        u2 tv[4], wq[4];
        const char* pin_ = scr + (64 * gq + col) * 32 + ((wv ^ (2 * ((col >> 3) & 1) + (gq & 1))) * 8);
#pragma unroll
        for (int e = 0; e < 4; ++e) tv[e] = *(const u2*)(pin_ + e * 512);
        const u4 m4 = *(const u4*)(scr + 26640);
        SB();
        u4 d0, d1;
        f4 r0, i0, r1, i1;
#define NOP1() do { asm volatile("s_nop 1"); SB(); } while (0)
        d0 = u4{__builtin_amdgcn_perm(tv[1].x, tv[0].x, 0x05040100u), __builtin_amdgcn_perm(tv[3].x, tv[2].x, 0x05040100u),
                __builtin_amdgcn_perm(tv[1].x, tv[0].x, 0x07060302u), __builtin_amdgcn_perm(tv[3].x, tv[2].x, 0x07060302u)};
        SB();
        d1 = u4{__builtin_amdgcn_perm(tv[1].y, tv[0].y, 0x05040100u), __builtin_amdgcn_perm(tv[3].y, tv[2].y, 0x05040100u),
                __builtin_amdgcn_perm(tv[1].y, tv[0].y, 0x07060302u), __builtin_amdgcn_perm(tv[3].y, tv[2].y, 0x07060302u)};
        SB();
        MZ(r0, d0, cm[0]); MZ(i0, d0, cm[1]); MZ(r1, d1, cm[0]); MZ(i1, d1, cm[1]);
        asm volatile("s_waitcnt vmcnt(%2)" : "+v"(wl[0]), "+v"(wl[1]) : "i"(24 - NAE) : "memory");
        *(u4*)(scr + 8192 + tid * 16) = wl[0]; *(u4*)(scr + 12288 + tid * 16) = wl[1];
        {
            const unsigned m01 = m4.x > m4.y ? m4.x : m4.y, m23 = m4.z > m4.w ? m4.z : m4.w;
            wexp = (unsigned)__builtin_amdgcn_readfirstlane((int)((m01 > m23 ? m01 : m23) >> 7));
            wexp = wexp > 248u ? 248u : wexp < 20u ? 20u : wexp;
        }
        const float wsc = __uint_as_float((250u - wexp) << 23);
        SB(); NOP1(); NOP1();
        d0 = cmul_cvt(r0, i0, twc, -tws);
        SB(); NOP1();
        MZ(r0, cm[0], d0); MZ(i0, cm[1], d0);
        d1 = cmul_cvt(r1, i1, twc, -tws);
        SB();
        __syncthreads();
#pragma unroll
        for (int e = 0; e < 4; ++e) wq[e] = *(const u2*)(pin_ + 8192 + e * 512);
        SB();
        MZ(r1, cm[0], d1); MZ(i1, cm[1], d1);
        r0 = r0 * wsc; i0 = i0 * wsc;
        SB();
        f4 wr0, wi0, wr1, wi1;
#pragma unroll
        for (int e = 0; e < 4; ++e) { wr0[e] = __uint_as_float(wq[e].x << 16); wi0[e] = __uint_as_float(wq[e].x & 0xffff0000u); }
        d0 = cmul_cvt(r0, i0, wr0, wi0);
        SB(); NOP1();
        MZ(r0, d0, cm[2]); MZ(i0, d0, cm[3]);
        r1 = r1 * wsc; i1 = i1 * wsc;
#pragma unroll
        for (int e = 0; e < 4; ++e) { wr1[e] = __uint_as_float(wq[e].y << 16); wi1[e] = __uint_as_float(wq[e].y & 0xffff0000u); }
        d1 = cmul_cvt(r1, i1, wr1, wi1);
        SB(); NOP1();
        MZ(r1, d1, cm[2]); MZ(i1, d1, cm[3]);
        d0 = cmul_cvt(r0, i0, twc, tws);
        SB(); NOP1();
        MZ(r0, cm[2], d0); MZ(i0, cm[3], d0);
        d1 = cmul_cvt(r1, i1, twc, tws);
        SB(); NOP1();
        MZ(r1, cm[2], d1); MZ(i1, cm[3], d1);
        u2 ow[4];
#pragma unroll
        for (int e = 0; e < 4; ++e) ow[e].x = pk_f16(cf{r0[e], i0[e]});
        SB();
        asm volatile("s_nop 3");
        SB();
        char* dst = scr + 16384 + (64 * gq + col) * 32 + ((wv ^ ((col >> 2) & 3)) * 8);
#pragma unroll
        for (int e = 0; e < 4; ++e) { ow[e].y = pk_f16(cf{r1[e], i1[e]}); *(u2*)(dst + e * 512) = ow[e]; }
        __syncthreads();
        const u4 o0 = *(const u4*)(scr + 16384 + tid * 16), o1 = *(const u4*)(scr + 20480 + tid * 16);
        __builtin_amdgcn_raw_buffer_store_b128(o0, rs, tid * 16 + WS_T2 + s0 * 8192, 0, 16);
        __builtin_amdgcn_raw_buffer_store_b128(o1, rs, tid * 16 + WS_T2 + 4096 + s0 * 8192, 0, 16);
    }
    asm volatile("s_waitcnt vmcnt(2)" ::: "memory");
    move_n<0, 0, 8>(traddr); A1(24, 0); move_n<1, 1, 8>(traddr); A1(25, 0); move_n<2, 2, 8>(traddr); A1(26, 0);
    move_n<3, 3, 8>(traddr); A1(27, 0); move_n<4, 4, 8>(traddr); A1(28, 0); move_n<5, 5, 8>(traddr); A1(29, 0);
    asm volatile("s_waitcnt vmcnt(%0) lgkmcnt(0)" :: "i"(6) : "memory");
    __builtin_amdgcn_s_barrier();
    publish(ws, 1, s0, tid, tag);
    if (b == 0 && tid == 0) __hip_atomic_store((GAS u64*)(ws + WS_NONCE), tag, RLX_AGENT);
    u2 bv[2]; unsigned bs;
    u4 ra, rb;
    {
        LD16(ra, xoff, x); LD16(rb, xoff, x + 256 * DN * 2);
        unsigned o = (unsigned)(nb1 * 64 + 16 * mb + 4 * gq) * 2u, ob = (unsigned)(nb2 * 64 + 16 * wv + col) * 2u;
        asm volatile("global_load_dwordx2 %0, %1, %2" : "=v"(bv[0]) : "v"(o), "s"(b1) : "memory");
        asm volatile("global_load_dwordx2 %0, %1, %2 offset:32" : "=v"(bv[1]) : "v"(o), "s"(b1) : "memory");
        asm volatile("global_load_ushort %0, %1, %2" : "=v"(bs) : "v"(ob), "s"(b2) : "memory");
    }
    const int pib = 32 * (lane >> 3) + 8 * ((lane >> 1) & 3) + 2 * wv + (lane & 1);
    const unsigned fb = (unsigned)(xcd * 4096 + pib * 16);
#define PB(k) do { if ((k) == POSB) POLL_ISSUE(pv, fb, 1); A1(k, (k) < 32 ? 0 : -8); } while (0)
    u4 cbr[4], cbi[4], ca3[2];
#define S3C(ks, e) do { const float rv = (float)((ka * (16 * (ks) + 4 * gq + (e))) & 63) * (1.f / 64); \
            const float c = cosr(rv) * 0.125f, sn = sinr(rv) * 0.125f; \
            cbr[ks][e] = pk_f16(cf{c, -sn}); cbi[ks][e] = pk_f16(cf{sn, c}); cbr[ks + 2][e] = cbr[ks][e] ^ sgk; cbi[ks + 2][e] = cbi[ks][e] ^ sgk; } while (0)
    PB(30); S3C(0, 0); PB(31); S3C(0, 1); PB(32); S3C(0, 2); PB(33); S3C(0, 3);
    PB(34); S3C(1, 0); PB(35); S3C(1, 1); PB(36); S3C(1, 2); PB(37); S3C(1, 3);
    PB(38);
#pragma unroll
    for (int f = 0; f < 2; ++f) {
        const int n1 = 4 * f + (col >> 2);
        const float rv = (float)(n1 * gq) * (1.f / 7), am = n1 < 7 ? (gq ? 2.f : 1.f) : 0.f;
        const unsigned pp = pk_f16(cf{cosr(rv) * am, -sinr(rv) * am});
        const unsigned pc = (col & 1) ? pp << 16 : pp & 0xffffu, ps = (col & 1) ? pp & 0xffff0000u : pp >> 16;
        ca3[f] = (col & 2) ? u4{0u, pc, 0u, ps} : u4{pc, 0u, ps, 0u};
        if (f == 0) PB(39);
    }
    PB(40);
    unsigned ya0, ya1, hxa, t2o, hr0, hpo;
    {
        int d0 = (64 * gq + 385 * ka) % DN;
        int d1 = d0 + 256; d1 = d1 >= DN ? d1 - DN : d1;
        if (gq == 3) d1 = DN + ka;
        ya0 = d0 * 8 + (d0 >> 3) * 16; ya1 = d1 * 8 + (d1 >> 3) * 16;
        hxa = lane * 128 + ((lane & 7) << 4) + wv * 4;
        PB(41);
        t2o = (unsigned)(((8 * gq + (ka >> 3)) * 256 + s0) * 32 + ((((ka >> 1) & 3) ^ ((s0 >> 2) & 3)) * 8) + (ka & 1) * 4);
        const int n = tid >> 4, d = 32 * (n >> 1) + 8 * (col >> 2) + 4 * (n & 1) + (col & 3);
        hr0 = (d >> 3) * 128 + (((d & 7) ^ ((d >> 3) & 7)) << 4);
        hpo = n * 1024 + col * 16 + WS_HP + (s0 >> 2) * (28 * 1024) + (s0 & 3) * 256;
        PIN(ya0); PIN(ya1); PIN(hxa); PIN(t2o); PIN(hr0); PIN(hpo);
    }
    PIN(cbr[0]); PIN(cbr[1]); PIN(cbr[2]); PIN(cbr[3]); PIN(cbi[0]); PIN(cbi[1]); PIN(cbi[2]); PIN(cbi[3]); PIN(ca3[0]); PIN(ca3[1]);
    PB(42); PB(43); PB(44); PB(45); PB(46); PB(47);
    asm volatile("s_waitcnt vmcnt(%6)" : "+v"(pv), "+v"(ra), "+v"(rb), "+v"(bv[0]), "+v"(bv[1]), "+v"(bs) : "i"(48 - POSB) : "memory");
    if (!poll_ok(pv, tag)) poll(ws, 1, xcd, pib, tag);
    {
        unsigned tq[8];
#pragma unroll
        for (int qq = 0; qq < 8; ++qq)
            asm volatile("global_load_dword %0, %1, %2 sc1" : "=v"(tq[qq]) : "v"(t2o), "s"(ws + WS_T2 + qq * (32 * 8192)) : "memory");
        A1(48, -8); move_n<6, 6, 8>(traddr); A1(49, -8); move_n<7, 7, 8>(traddr); A1(50, -8);
        cf xv[8];
        PIN(tsc); PIN(tss);
#pragma unroll
        for (int q = 1; q < 8; ++q) ts[q] = mktw(cf{rdlane(tsc, q), rdlane(tss, q)});
        A1(51, -8);
#pragma unroll
        for (int i = 0; i < 4; ++i) {
            xv[2 * i] = cf{__uint_as_float(ra[i] << 16), __uint_as_float(rb[i] << 16)};
            xv[2 * i + 1] = cf{__uint_as_float(ra[i] & 0xffff0000u), __uint_as_float(rb[i] & 0xffff0000u)};
            if (i == 1) A1(52, -8);
        }
        A1(53, -8);
        asm volatile("s_waitcnt vmcnt(22)" ::: "memory");
        move_n<8, 0, 8>(traddr); A1(54, -8); move_n<9, 1, 8>(traddr); A1(55, -8);
        asm volatile("s_waitcnt vmcnt(8) lgkmcnt(0)" : "+v"(tq[0]), "+v"(tq[1]), "+v"(tq[2]), "+v"(tq[3]), "+v"(tq[4]), "+v"(tq[5]), "+v"(tq[6]), "+v"(tq[7]) :: "memory");
        cf rr[8];
#pragma unroll
        for (int qq = 0; qq < 8; ++qq) rr[qq] = unpk_f16(tq[qq]);
        A1(56, -8);
        {
            cf tm[8];
#pragma unroll
            for (int qq = 1; qq < 8; ++qq) tm[qq] = rr[qq] * ts[qq].w.xx;
            SB();
#pragma unroll
            for (int qq = 1; qq < 8; ++qq) rr[qq] = cfma(rr[qq].yx, ts[qq].n, tm[qq]);
        }
        A1(57, -8);
        dft8<true>(rr);
        A1(58, -8);
        {
            char* dst = scr + gq * (4 * 288) + ka * 4;
            unsigned pz[8];
#pragma unroll
            for (int j = 0; j < 8; ++j) pz[j] = pk_f16(rr[j]);
            SB();
#pragma unroll
            for (int j = 0; j < 8; ++j) *(unsigned*)(dst + (16 * (j >> 2) + (j & 3)) * 288) = pz[j];
        }
        A1(59, -8);
        __syncthreads();
        A1(60, -8);
        u4 bop[2];
        f4 y00, y01, y10, y11;
        {
            u4 a0[4], a1[4];
#pragma unroll
            for (int ks = 0; ks < 4; ++ks) a0[ks] = *(const u4*)(scr + col * 288 + ks * 64 + gq * 16);
#pragma unroll
            for (int ks = 0; ks < 4; ++ks) a1[ks] = *(const u4*)(scr + (16 + col) * 288 + ks * 64 + gq * 16);
            A1(61, -8);
            f4 re0, im0, re1, im1;
            MZ(re0, a0[0], cbr[0]); MZ(im0, a0[0], cbi[0]); MA(re0, a0[1], cbr[1]); MA(im0, a0[1], cbi[1]);
            A1(62, -8);
            MA(re0, a0[2], cbr[2]); MA(im0, a0[2], cbi[2]); MA(re0, a0[3], cbr[3]); MA(im0, a0[3], cbi[3]);
            MZ(re1, a1[0], cbr[0]); MZ(im1, a1[0], cbi[0]);
            A1(63, -8);
            MA(re1, a1[1], cbr[1]); MA(im1, a1[1], cbi[1]); MA(re1, a1[2], cbr[2]); MA(im1, a1[2], cbi[2]);
            bop[0] = cvt8(re0, im0);
            SB();
            MA(re1, a1[3], cbr[3]); MA(im1, a1[3], cbi[3]);
            MZ(y00, ca3[0], bop[0]); MZ(y01, ca3[1], bop[0]);
            A2(0, 0);
            asm volatile("s_nop 2");
            SB();
            bop[1] = cvt8(re1, im1);
            SB(); NOP1();
            MZ(y10, ca3[0], bop[1]); MZ(y11, ca3[1], bop[1]);
        }
        char* yx = scr + 32 * 288;
        *(cf*)(yx + ya0) = y00.xy; *(cf*)(yx + ya0 + 5120) = y00.zw; *(cf*)(yx + ya1) = y01.xy; *(cf*)(yx + ya1 + 5120) = y01.zw;
        SB();
        asm volatile("s_nop 5");
        SB();
        *(cf*)(yx + ya0 + 10240) = y10.xy; *(cf*)(yx + ya0 + 15360) = y10.zw; *(cf*)(yx + ya1 + 10240) = y11.xy; *(cf*)(yx + ya1 + 15360) = y11.zw;
        A2(1, 0);
        __syncthreads();
        A2(2, 0);
        const float ysc = __uint_as_float((wexp + 2u) << 23) * (32.0f * 256.0f / ((float)SN * (float)DN));
        f4 yy[4];
#pragma unroll
        for (int i = 0; i < 4; ++i) yy[i] = *(const f4*)(yx + wv * 5120 + lc * 80 + i * 16);
        A2(3, 0);
        cf v[8];
#pragma unroll
        for (int i = 0; i < 4; ++i) {
            v[2 * i] = cfma(yy[i].xy, cf{ysc, ysc}, xv[2 * i]);
            v[2 * i + 1] = cfma(yy[i].zw, cf{ysc, ysc}, xv[2 * i + 1]);
        }
        A2(4, 0);
        u4 ha, hb;
#pragma unroll
        for (int i = 0; i < 4; ++i) { ha[i] = pk_bf16(cf{v[2 * i].x, v[2 * i + 1].x}); hb[i] = pk_bf16(cf{v[2 * i].y, v[2 * i + 1].y}); }
        A2(5, 0);
#pragma unroll
        for (int i = 0; i < 4; ++i) {
            v[2 * i] = cf{__uint_as_float(ha[i] << 16), __uint_as_float(hb[i] << 16)};
            v[2 * i + 1] = cf{__uint_as_float(ha[i] & 0xffff0000u), __uint_as_float(hb[i] & 0xffff0000u)};
            if (i == 1) SB();
        }
        A2(6, 0);
        cf mean, var;
        row_stats<8, true>(v, lane < 56 ? 1.f : 0.f, mean, var);
        A2(7, 0);
        __builtin_amdgcn_raw_buffer_store_b128(ha, rs, xoff + WS_H, 0, 16);
        const cf r1 = rsq2(var + 1e-5f);
#pragma unroll
        for (int e = 0; e < 8; ++e) v[e] = v[e] - mean;
        const cf rr2 = r1 * rsq2(cfma(var * r1, r1, cf{1e-5f, 1e-5f}));
        SB();
        __builtin_amdgcn_raw_buffer_store_b128(hb, rs, xoff + (WS_H + 256 * DN * 2), 0, 16);
#pragma unroll
        for (int e = 0; e < 8; ++e) v[e] = v[e] * rr2;
        SB();
        char* hx = scr;
#pragma unroll
        for (int e = 0; e < 8; ++e) *(unsigned*)(hx + (hxa ^ (e << 4))) = pk_bf16(v[e]);
        __syncthreads();
        const u4 p0 = *(const u4*)(hx + hr0), p1 = *(const u4*)(hx + hr0 + 4096);
        __builtin_amdgcn_raw_buffer_store_b128(p0, rs, hpo, 0, 16);
        if (wv < 3) __builtin_amdgcn_raw_buffer_store_b128(p1, rs, hpo + 16 * 1024, 0, 16);
    }
    asm volatile("s_waitcnt vmcnt(3)" ::: "memory");
    move_n<10, 2, 24>(traddr);
    asm volatile("s_waitcnt lgkmcnt(8)" ::: "memory");
    A2(8, 0); move_n<13, 5, 8>(traddr); A2(9, 0); move_n<14, 6, 8>(traddr); A2(10, 0); move_n<15, 7, 8>(traddr); A2(11, 0);
    asm volatile("s_waitcnt vmcnt(4) lgkmcnt(0)" ::: "memory");
    __builtin_amdgcn_s_barrier();
    publish(ws, 2, s0, tid, tag);
    N2(12, POSC - 12, 0);
    {
        const unsigned fc = (unsigned)(xcd * 4096 + tid * 16);
        POLL_ISSUE(pv, fc, 2);
        N2(POSC, 32 - POSC, 0);
        asm volatile("s_waitcnt vmcnt(%1)" : "+v"(pv) : "i"(32 - POSC) : "memory");
    }
    f4 acc[4][2];
    char* redw = scr + wv * 6144 + lane * 16;
#define RED_W(c, fa, fb) do { *(f4*)(redw + ((c) - 1) * 2048) = fa; *(f4*)(redw + ((c) - 1) * 2048 + 1024) = fb; } while (0)
#define RED_R(fa, fb) do { f4 pr_[3][2]; \
        _Pragma("unroll") for (int c = 1; c < 4; ++c) { const char* src_ = scr + (wv ^ c) * 6144 + (c - 1) * 2048 + lane * 16; \
            pr_[c - 1][0] = *(const f4*)src_; pr_[c - 1][1] = *(const f4*)(src_ + 1024); } \
        SB(); \
        fa = (fa + pr_[0][0]) + (pr_[1][0] + pr_[2][0]); fb = (fb + pr_[0][1]) + (pr_[1][1] + pr_[2][1]); } while (0)
    const int fi = db2 * 28 + wv * 7 + (lane < 7 ? lane : 0);
    const unsigned fo = (unsigned)(xcd * 4096 + fi * 16);
    const unsigned bo0 = (unsigned)lane * 16u, bo1 = bo0 + 1024u;
    const char* ab2 = ws + WS_UP + db2 * 4096 + (size_t)wv * (14 * 28 * 1024);
    u4 bfr2[28][2];
    if (has1) {
        const unsigned ao0 = (unsigned)lane * 16u + (wv & 1) * 1024u, ao1 = ao0 ^ 1024u;
        const char* ab = ws + WS_HP + db1 * 4096 + (size_t)wv * (16 * 28 * 1024);
        const int uo = lane * 16 + WS_UP + ((nb1 * 2 + (wv >> 1)) * 28 + db1 * 4 + (wv & 1)) * 1024;
        cf bias[4];
#pragma unroll
        for (int k = 0; k < 4; ++k) bias[k] = unpk_bf16(bv[k >> 1][k & 1]);
        u4 bfr[32][2];
        if (!poll_ok(pv, tag)) poll(ws, 2, xcd, tid, tag);
        act_issue<16, 0, GPD>(bfr, ab, ao0, ao1);
        gemm_mfma<true, 16, GPD, 0, 32, 0, 0, 0, 0, -1, false, 0, 0>(acc, bfr, ab, ao0, ao1, traddr2, [] {}, [](auto) {});
#define EPI1(X, half, MID) do { \
        X(0); RED_W(1, acc[0][1], acc[1][1]); X(1); RED_W(2, acc[2][0], acc[3][0]); X(2); RED_W(3, acc[2][1], acc[3][1]); X(3); \
        __syncthreads(); \
        MID; \
        RED_R(acc[0][0], acc[1][0]); \
        X(4); \
        cf vv[4] = {acc[0][0].xy + bias[0], acc[0][0].zw + bias[1], acc[1][0].xy + bias[2], acc[1][0].zw + bias[3]}, zz[4]; \
        const float KA = -2.0f * 0.7978845608028654f * 1.4426950408889634f; \
        _Pragma("unroll") for (int k = 0; k < 4; ++k) zz[k] = vv[k] * vv[k]; \
        SB(); \
        _Pragma("unroll") for (int k = 0; k < 4; ++k) zz[k] = cfma(zz[k], cf{0.044715f * KA, 0.044715f * KA}, cf{KA, KA}); \
        SB(); \
        _Pragma("unroll") for (int k = 0; k < 4; ++k) zz[k] = zz[k] * vv[k]; \
        X(5); \
        _Pragma("unroll") for (int k = 0; k < 4; ++k) zz[k] = cf{__builtin_amdgcn_exp2f(zz[k].x), __builtin_amdgcn_exp2f(zz[k].y)}; \
        X(6); \
        _Pragma("unroll") for (int k = 0; k < 4; ++k) zz[k] = zz[k] + 1.0f; \
        SB(); \
        _Pragma("unroll") for (int k = 0; k < 4; ++k) zz[k] = cf{__builtin_amdgcn_rcpf(zz[k].x), __builtin_amdgcn_rcpf(zz[k].y)}; \
        X(7); \
        _Pragma("unroll") for (int k = 0; k < 4; ++k) vv[k] = vv[k] * zz[k]; \
        SB(); \
        __builtin_amdgcn_raw_buffer_store_b128(u4{pk_bf16(vv[0]), pk_bf16(vv[1]), pk_bf16(vv[2]), pk_bf16(vv[3])}, rs, uo + (half) * 2048, 0, 16); \
        SB(); } while (0)
#define XA(k) do { SB(); ACT_LD(bfr[16 + GPD + ((k) >> 1)][(k) & 1], ((k) & 1) ? ao1 : ao0, act_ptr<16>(ab, 16 + GPD + ((k) >> 1))); SB(); } while (0)
        EPI1(XA, 0, (void)0);
        constexpr int PDB = GPD + 4, PE = 32 + 2 * PDB;
        gemm_mfma<true, 16, PDB, 16, 32, 1, 1, 8, 0, HK1, true, 0, 0>(acc, bfr, ab, ao0, ao1, traddr2, [&] {
            asm volatile("s_waitcnt vmcnt(%0)" :: "i"(2 * HK1) : "memory");
            __builtin_amdgcn_s_barrier();
            publish(ws, 3, db1 * 28 + nb1, tid, tag);
        }, [&](auto ic) { constexpr int P = 32 + decltype(ic)::value; stage1<false, P, (P >> 2) & 7>(w2w, v2o[0], v2o[1], ldsbase);
                          if (P == PPOS) poll_issue(pv, fo, ws, 3); });
        static_assert(PE == 56, "all of w2 is staged by the end of GEMM1");
#define XB(k) do { SB(); if ((k) == XB0) { asm volatile("s_waitcnt vmcnt(%1)" : "+v"(pv) : "i"(55 - PPOS) : "memory"); if (!poll_ok(pv, tag)) poll(ws, 3, xcd, fi, tag); } \
        if ((k) >= XB0 && (k) < XB0 + GPD2) act_issue<14, ((k) >= XB0 ? (k) - XB0 : 0), 1>(bfr2, ab2, bo0, bo1); SB(); } while (0)
        static_assert(XB0 + GPD2 <= 8, "");
        EPI1(XB, 1, (void)0);
        asm volatile("s_waitcnt vmcnt(%0)" :: "i"(2 * GPD2 + 1) : "memory");
        move_n<8, 0, 48>(traddr2);
    } else {
        if (off >= 25) {
            const u4 s2 = make_srd(w2, SN * FN * 2);
            const unsigned vp = (unsigned)(448 * wv + 64 * NP2 + 2 * (off - 25) + (lane >> 5)) * (SN * 2) + xcd * 512 + (lane & 31) * 16;
#pragma unroll 1
            for (int i = 0; 64 * NP2 + 14 * i + 2 * (off - 25) < 448; ++i) GLDS_F(s2, vp, (unsigned)(i * 14 * SN * 2), dump, "");
        }
        if (!has2 && tid < 64) __builtin_amdgcn_raw_buffer_store_b128(clr, rs, WS_FLAGS + (b - 224) * 1024 + tid * 16, 0, 16);
        DRAIN();
        if (!has2) return;
        move_n<0, 0, 64>(traddr2);
        LGKM0();
        N2(32, 24, -8);
        DRAIN();
        move_n<8, 0, 48>(traddr2);
        if (off < 25) __builtin_amdgcn_s_sleep(ISL); else __builtin_amdgcn_s_sleep(ISL2);
        poll(ws, 3, xcd, fi, tag);
        act_issue<14, 0, GPD2>(bfr2, ab2, bo0, bo1);
    }
    {
        const unsigned ho = (unsigned)((nb2 * 64 + 16 * wv + col) * DN + db2 * 64 + 8 * gq) * 2u;
        u4 hv[2];
        gemm_mfma<false, 14, GPD2, 0, 14, 0, 0, 0, 0, HK2, false, 0, 0, 3, HKB>(acc, bfr2, ab2, bo0, bo1, traddr2, [&] {
            asm volatile("global_load_dwordx4 %0, %1, %2 sc1" : "=v"(hv[0]) : "v"(ho), "s"(ws + WS_H) : "memory");
            asm volatile("global_load_dwordx4 %0, %1, %2 offset:64 sc1" : "=v"(hv[1]) : "v"(ho), "s"(ws + WS_H) : "memory");
            POLL_ISSUE(pv, fo, 4);
        }, [&](auto) {
            asm volatile("s_waitcnt vmcnt(%0) lgkmcnt(0)" :: "i"(2 * HKB) : "memory");
            __builtin_amdgcn_s_barrier();
            if (has1) publish(ws, 4, db1 * 28 + nb1, tid, tag);
        });
        const float bb = __uint_as_float(bs << 16);
        const auto ro = __builtin_amdgcn_make_buffer_rsrc(out, 0, SN * DN * 2, 0x00020000);
#define EPI2(half, MID) do { \
        RED_W(1, acc[1][0], acc[1][1]); RED_W(2, acc[2][0], acc[2][1]); RED_W(3, acc[3][0], acc[3][1]); \
        SB(); MID; SB(); \
        cf hb2[4]; \
        _Pragma("unroll") for (int k = 0; k < 4; ++k) hb2[k] = unpk_bf16(hv[half][k]) + bb; \
        __syncthreads(); \
        RED_R(acc[0][0], acc[0][1]); \
        __builtin_amdgcn_raw_buffer_store_b128(u4{pk_bf16(acc[0][0].xy + hb2[0]), pk_bf16(acc[0][0].zw + hb2[1]), pk_bf16(acc[0][1].xy + hb2[2]), pk_bf16(acc[0][1].zw + hb2[3])}, ro, ho + (half) * 64, 0, 0); \
        SB(); } while (0)
#define MID2 do { asm volatile("s_waitcnt vmcnt(0)" : "+v"(hv[0]), "+v"(hv[1]), "+v"(pv) :: "memory"); \
        if (!poll_ok(pv, tag)) poll(ws, 4, xcd, fi, tag); \
        act_issue<14, 14, GPD2>(bfr2, ab2, bo0, bo1); } while (0)
        EPI2(0, MID2);
        gemm_mfma<false, 14, GPD2, 14, 28, 1, 0, 0, 0, 4, false, 0, 0>(acc, bfr2, ab2, bo0, bo1, traddr2, [] { __builtin_amdgcn_s_barrier(); }, [](auto) {});
        EPI2(1, (void)0);
    }
    asm volatile(".Lkf_code_end:" ::: "memory");
}

extern "C" void kernel_launch(void* const* d_in, const int* in_sizes, int n_in,
                              void* d_out, int out_size, void* d_ws, size_t ws_size,
                              hipStream_t stream) {
    (void)hipFuncSetAttribute((const void*)kf_fused, hipFuncAttributeMaxDynamicSharedMemorySize, LDS_TOTAL);
    kf_fused<<<256, 256, LDS_TOTAL, stream>>>((char*)d_ws, (const char*)d_in[0], (const char*)d_in[1], (const char*)d_in[2],
                                              (const char*)d_in[4], (const char*)d_in[5], (const char*)d_in[6], (const char*)d_in[7], (u16*)d_out);
}
```
